```python
import torch

def _softmax(x, axis=-1):
    return torch.softmax(x, dim=axis)
def _gelu(x):
    return torch.nn.functional.gelu(x, approximate="tanh")
def _silu(x):
    return torch.nn.functional.silu(x)
def _sigmoid(x):
    return torch.sigmoid(x)
def _layernorm(x, axis=-1, eps=1e-5):
    m = x.mean(dim=axis, keepdim=True)
    v = x.var(dim=axis, keepdim=True, unbiased=False)
    return (x - m) / torch.sqrt(v + eps)
def _scatter_add(tgt, idx, src, axis=0):
    return tgt.clone().index_add_(axis, idx, src)
def _segment_sum(src, seg, n):
    out = torch.zeros((n,) + src.shape[1:], dtype=src.dtype)
    return out.index_add_(0, seg, src)
def _pad(x, pw):
    flat = tuple(v for pair in reversed(pw) for v in pair)
    return torch.nn.functional.pad(x, flat)

kernel_name = 'interleave_layers__mha_block__glu_gated_bilinear__prompt'

def setup_inputs():

    _w = (lambda a: (lambda t: t.to(torch.bfloat16) if t.is_floating_point() else t)(torch.as_tensor(a)))
    def _A_setup():
        B, S, D, H = 1, 2048, 512, 32
        return {
            "x": _w((lambda *s: torch.randn(*s, dtype=torch.bfloat16) * 0.05)(B, S, D)),
            "wqkv": _w((lambda *s: torch.randn(*s, dtype=torch.bfloat16) * 0.05)(D, 3 * D)),
            "wo": _w((lambda *s: torch.randn(*s, dtype=torch.bfloat16) * 0.05)(D, D)),
        }
    _A_in = _A_setup()

    def _B_setup():
        B, S, D, F = 1, 2048, 512, 1792
        return {
            "x": _w((lambda *s: torch.randn(*s, dtype=torch.bfloat16) * 0.05)(B, S, D)),
            "w_gate": _w((lambda *s: torch.randn(*s, dtype=torch.bfloat16) * 0.4)(D, F) * (D ** -0.5)),
            "w_up": _w((lambda *s: torch.randn(*s, dtype=torch.bfloat16) * 0.4)(D, F) * (D ** -0.5)),
            "w_down": _w((lambda *s: torch.randn(*s, dtype=torch.bfloat16) * 0.4)(F, D) * (F ** -0.5)),
        }
    _B_in = _B_setup()

    return {
        "A_x": _A_in["x"],
        "A_wqkv": _A_in["wqkv"],
        "A_wo": _A_in["wo"],
        "B_x": _B_in["x"],
        "B_w_gate": _B_in["w_gate"],
        "B_w_up": _B_in["w_up"],
        "B_w_down": _B_in["w_down"],
    }

def reference(A_x, A_wqkv, A_wo, B_x, B_w_gate, B_w_up, B_w_down):
    def _A_ref(x, wqkv, wo):
        B, S, D = x.shape
        H = 32
        Dh = D // H
        qkv = (lambda x, *d: x.permute(*d))((x @ wqkv).reshape(B, S, 3, H, Dh), 2, 0, 3, 1, 4)
        q, k, v = qkv[0], qkv[1], qkv[2]
        scores = torch.einsum("bhqd,bhkd->bhqk", q, k) * (Dh ** -0.5)
        p = _softmax(scores, axis=-1)
        o = torch.einsum("bhqk,bhkd->bhqd", p, v)
        return (lambda x, *d: x.permute(*d))(o, 0, 2, 1, 3).reshape(B, S, D) @ wo

    def _B_ref(x, w_gate, w_up, w_down):
        g = torch.einsum("bsd,df->bsf", x, w_gate)
        u = torch.einsum("bsd,df->bsf", x, w_up)
        h = g * u
        return torch.einsum("bsf,fd->bsd", h, w_down)

    x = A_x
    x = x + _A_ref(_layernorm(x, axis=-1), A_wqkv, A_wo)
    x = x + _B_ref(_layernorm(x, axis=-1), B_w_gate, B_w_up, B_w_down)
    x = x + _A_ref(_layernorm(x, axis=-1), A_wqkv, A_wo)
    x = x + _B_ref(_layernorm(x, axis=-1), B_w_gate, B_w_up, B_w_down)
    return x
```

```cpp
#include <hip/hip_runtime.h>
#include <hip/hip_bf16.h>
#include <stdint.h>

#pragma clang diagnostic ignored "-Winline-asm"

typedef __attribute__((ext_vector_type(2))) unsigned u32x2;
typedef __attribute__((ext_vector_type(4))) unsigned u32x4;
typedef __attribute__((ext_vector_type(2))) float f32x2;
typedef __attribute__((ext_vector_type(4))) float f32x4;
typedef __attribute__((ext_vector_type(8))) float f32x8;
typedef __attribute__((ext_vector_type(16))) float f32x16;
typedef __attribute__((ext_vector_type(4))) short s16x4;
typedef __bf16 bf16x2 __attribute__((ext_vector_type(2)));
typedef __bf16 bf16x8 __attribute__((ext_vector_type(8)));
typedef __bf16 bf16x16 __attribute__((ext_vector_type(16)));
typedef __amdgpu_buffer_rsrc_t rsrc_t;
typedef unsigned long long u64;
typedef __attribute__((address_space(1))) u64 gu64;
#define LAS __attribute__((address_space(3)))
#define DI __device__ __forceinline__
#define SB() __builtin_amdgcn_sched_barrier(0)
#define PIN(x) asm volatile("" : "+v"(x))
#define RLX_AGENT __ATOMIC_RELAXED, __HIP_MEMORY_SCOPE_AGENT

namespace {

constexpr int SEQ = 2048, DM = 512, FF = 1792;
constexpr int SC1 = 16;

constexpr unsigned MB = 1u << 20;
constexpr unsigned OFF_EPOCH = 0;
constexpr unsigned OFF_FQKV = 4096;
constexpr unsigned OFF_FO = OFF_FQKV + 2 * 16 * 64 * 8;
constexpr unsigned OFF_FX1 = OFF_FO + 2 * 64 * 32 * 8;
constexpr unsigned OFF_FH = OFF_FX1 + 2 * 64 * 16 * 8;
constexpr unsigned OFF_FX2 = OFF_FH + 2 * 64 * 16 * 8;
constexpr unsigned OFF_FW = OFF_FX2 + 64 * 16 * 8;
constexpr unsigned OFF_CSG = 128 * 1024;
constexpr unsigned OFF_ST1 = 256 * 1024;
constexpr unsigned OFF_ST2 = OFF_ST1 + 2 * 256 * 1024;
constexpr unsigned OFF_WO = 1 * MB;
constexpr unsigned OFF_WD = OFF_WO + 512 * 1024;
constexpr unsigned OFF_WG = OFF_WD + 1792 * 1024;
constexpr unsigned OFF_Q = 8 * MB;
constexpr unsigned OFF_K = OFF_Q + 4 * MB;
constexpr unsigned OFF_V = OFF_K + 4 * MB;
constexpr unsigned OFF_O = OFF_V + 4 * MB;
constexpr unsigned OFF_X1 = OFF_O + 4 * MB;
constexpr unsigned OFF_X2 = OFF_X1 + 4 * MB;
constexpr unsigned OFF_H = OFF_X2 + 2 * MB;
constexpr unsigned OFF_DBG = OFF_H + 14 * MB;
constexpr unsigned WS_USED = OFF_DBG + 1 * MB;

constexpr unsigned L_SLOT = 32768;
constexpr unsigned L_V = 65536;
constexpr unsigned L_XR = 98304;
constexpr unsigned L_EX = 131072;
constexpr unsigned L_CSQ = 147456;
constexpr unsigned L_MU = L_CSQ + 512;
constexpr unsigned L_RS = L_MU + 512;
constexpr unsigned L_CSG = L_RS + 512;
constexpr unsigned L_CSP = L_CSG + 1024;
constexpr unsigned LDS_BYTES = 163840;

template <int I> struct IC { static constexpr int v = I; };
template <int B, int E, class Fn> DI void static_for(Fn&& f) {
    if constexpr (E - B == 1) f(IC<B>{});
    else if constexpr (E - B > 1) { static_for<B, (B + E) / 2>(f); static_for<(B + E) / 2, E>(f); }
}
template <class T, class U> DI T bc(U v) { return __builtin_bit_cast(T, v); }
DI unsigned rfl(unsigned v) { return __builtin_amdgcn_readfirstlane(v); }
DI unsigned lane_id() { unsigned l; asm volatile("v_mbcnt_lo_u32_b32 %0, -1, 0\n\tv_mbcnt_hi_u32_b32 %0, -1, %0" : "=v"(l)); return l; }
DI rsrc_t mkrsrc(const void* p, unsigned bytes) { return __builtin_amdgcn_make_buffer_rsrc((void*)p, (short)0, (int)bytes, 0x00020000); }
DI f32x16 mfma32(u32x4 a, u32x4 b, f32x16 c) { return __builtin_amdgcn_mfma_f32_32x32x16_bf16(bc<bf16x8>(a), bc<bf16x8>(b), c, 0, 0, 0); }
DI f32x4 mfma16(u32x4 a, u32x4 b, f32x4 c) { return __builtin_amdgcn_mfma_f32_16x16x32_bf16(bc<bf16x8>(a), bc<bf16x8>(b), c, 0, 0, 0); }
DI f32x16 smfmac(u32x4 a, f32x8 b, f32x16 c, int idx) { return __builtin_amdgcn_smfmac_f32_32x32x32_bf16(bc<bf16x8>(a), bc<bf16x16>(b), c, idx, 0, 0); }
DI unsigned pk2(float lo, float hi) { bf16x2 v = {(__bf16)lo, (__bf16)hi}; return bc<unsigned>(v); }
DI float bflo(unsigned d) { return bc<float>(d << 16); }
DI float bfhi(unsigned d) { return bc<float>(d & 0xffff0000u); }
DI float swap32_add(float v) { auto r = __builtin_amdgcn_permlane32_swap(bc<unsigned>(v), bc<unsigned>(v), false, false); return bc<float>(r[0]) + bc<float>(r[1]); }
DI float swap32_max(float v) { auto r = __builtin_amdgcn_permlane32_swap(bc<unsigned>(v), bc<unsigned>(v), false, false); return fmaxf(bc<float>(r[0]), bc<float>(r[1])); }

template <class T> DI T lds_ld(unsigned a) { return *(const LAS T*)(size_t)a; }
template <class T> DI void lds_st(unsigned a, T v) { *(LAS T*)(size_t)a = v; }
DI void lds_add(unsigned a, float v) { __hip_atomic_fetch_add((LAS float*)(size_t)a, v, __ATOMIC_RELAXED, __HIP_MEMORY_SCOPE_WORKGROUP); }
DI u32x2 lds_tr(unsigned a) { return bc<u32x2>(__builtin_amdgcn_ds_read_tr16_b64_v4i16((LAS s16x4*)(size_t)a)); }

template <int AUX> DI u32x4 ld16(rsrc_t r, unsigned vo, unsigned so) { return __builtin_amdgcn_raw_buffer_load_b128(r, vo, so, AUX); }
template <int AUX> DI u32x2 ld8(rsrc_t r, unsigned vo, unsigned so) { return __builtin_amdgcn_raw_buffer_load_b64(r, vo, so, AUX); }
template <int AUX> DI void st16(u32x4 v, rsrc_t r, unsigned vo, unsigned so) { __builtin_amdgcn_raw_buffer_store_b128(v, r, vo, so, AUX); }
template <int AUX> DI void st8(u32x2 v, rsrc_t r, unsigned vo, unsigned so) { __builtin_amdgcn_raw_buffer_store_b64(v, r, vo, so, AUX); }

DI u32x4 ldfrag(rsrc_t r, unsigned vo, unsigned so, int i) { return ld16<SC1>(r, vo + (i & 3) * 1024, so + (i >> 2) * 4096); }
DI void stfrag(u32x4 v, rsrc_t r, unsigned vo, unsigned so, int i) { st16<SC1>(v, r, vo + (i & 3) * 1024, so + (i >> 2) * 4096); }

template <bool DEV, int IMM> DI void dma(rsrc_t r, unsigned vo, unsigned so, unsigned la) {
    if constexpr (DEV) asm volatile("s_mov_b32 m0, %0\n\ts_nop 0\n\tbuffer_load_dwordx4 %1, %2, %3 offen offset:%4 sc1 lds" ::"s"(la), "v"(vo), "s"(r), "s"(so), "i"(IMM) : "m0", "memory");
    else asm volatile("s_mov_b32 m0, %0\n\ts_nop 0\n\tbuffer_load_dwordx4 %1, %2, %3 offen offset:%4 lds" ::"s"(la), "v"(vo), "s"(r), "s"(so), "i"(IMM) : "m0", "memory");
}
template <int N> DI void vmwait() { asm volatile("s_waitcnt vmcnt(%0)" ::"i"(N) : "memory"); }
DI void bar() { SB(); asm volatile("s_barrier" ::: "memory"); SB(); }
DI void bar_lds() { SB(); asm volatile("s_waitcnt lgkmcnt(0)\n\ts_barrier" ::: "memory"); SB(); }

DI void wait_flags(char* ws, unsigned off, u64 tag) {
    gu64* p = (gu64*)(ws + off);
    for (unsigned sp = 0; sp < (1u << 18); ++sp) {
        const u64 v = __hip_atomic_load(p, RLX_AGENT);
        if (__all(v == tag)) break;
    }
    asm volatile("" ::: "memory");
}
DI void set_flag(char* ws, unsigned off, u64 tag, unsigned lane) {
    vmwait<0>();
    if (lane == 0) __hip_atomic_store((gu64*)(ws + off), tag, RLX_AGENT);
}

#define STAMP(i)

struct Ctx {
    char* ws;
    rsrc_t rws, rx, rwqkv;
    u64 tag;
    unsigned b, g, r, w;
};
DI Ctx fresh(const Ctx& c0) {
    Ctx c = c0;
    asm volatile("" : "+s"(c.b), "+s"(c.w));
    c.g = c.b & 7; c.r = c.b >> 3;
    return c;
}

DI void load_stats(const Ctx& c, unsigned off, unsigned tt, unsigned lane, u32x2 (&sp)[8]) {
    const unsigned vo = (lane & 31) * 8 + (lane >> 5) * 2048;
#pragma unroll
    for (int i = 0; i < 8; ++i) sp[i] = ld8<SC1>(c.rws, vo + i * 256, off + tt * 4096);
}
DI void finish_stats(float s, float q, float& mu, float& rs) {
    s = swap32_add(s); q = swap32_add(q);
    mu = s * (1.f / DM);
    const float var = fmaxf(q * (1.f / DM) - mu * mu, 0.f);
    rs = __builtin_amdgcn_rsqf(var + 1e-5f);
}
DI void reduce_stats(const u32x2 (&sp)[8], float& mu, float& rs) {
    float s = 0.f, q = 0.f;
#pragma unroll
    for (int i = 0; i < 8; ++i) { s += bc<float>(sp[i].x); q += bc<float>(sp[i].y); }
    finish_stats(s, q, mu, rs);
}

template <int C> DI void qkv_w_chunk(const Ctx& c, unsigned wbase, unsigned part, unsigned lane) {
    const unsigned vo = (lane >> 2) * 3072 + (lane & 3) * 16, j = c.r & 15;
#pragma unroll
    for (int q = 0; q < 6; ++q) {
        const unsigned p = part * 6 + q, s = p >> 3, rg = p & 7;
        dma<false, 0>(c.rwqkv, vo, (128 * C + 16 * rg) * 3072 + s * 1024 + j * 64, wbase + C * 24576 + p * 1024);
    }
}
template <bool FIRST> DI u32x4 qkv_wfrag(const unsigned (&wl)[2], int kb, int s) {
    const unsigned a = wl[kb >> 4] + ((kb >> 3) & 1) * 24576 + s * 8192 + (kb & 7) * 1024;
    const u32x2 lo = lds_tr(a), hi = lds_tr(a + (FIRST ? 256 : 512));
    return u32x4{lo.x, lo.y, hi.x, hi.y};
}

DI void qkv_epilogue(const Ctx& c, unsigned layer, unsigned tt, unsigned lane, const f32x16& aq, const f32x16& ak, const f32x16& av) {
    const unsigned t = lane & 31, h = lane >> 5, j = c.r & 15, tl = (c.w & 3) * 32;
    const float mu = lds_ld<float>(L_MU + (tl + t) * 4), rs = lds_ld<float>(L_RS + (tl + t) * 4);
    const float rq = rs * (0.25f * 1.4426950408889634f);
    const float csv = lds_ld<float>(L_CSQ + (64 + t) * 4);
    unsigned pq[8], pk[8], pv[8];
#pragma unroll
    for (int q = 0; q < 4; ++q) {
        const f32x4 cq = lds_ld<f32x4>(L_CSQ + (8 * q + 4 * h) * 4), ck = lds_ld<f32x4>(L_CSQ + (32 + 8 * q + 4 * h) * 4);
        const f32x4 m4 = lds_ld<f32x4>(L_MU + (tl + 8 * q + 4 * h) * 4), r4 = lds_ld<f32x4>(L_RS + (tl + 8 * q + 4 * h) * 4);
#pragma unroll
        for (int e = 0; e < 2; ++e) {
            const int a = 4 * q + 2 * e;
            pq[2 * q + e] = pk2((aq[a] - mu * cq[2 * e]) * rq, (aq[a + 1] - mu * cq[2 * e + 1]) * rq);
            pk[2 * q + e] = pk2((ak[a] - mu * ck[2 * e]) * rs, (ak[a + 1] - mu * ck[2 * e + 1]) * rs);
            pv[2 * q + e] = pk2((av[a] - m4[2 * e] * csv) * r4[2 * e], (av[a + 1] - m4[2 * e + 1] * csv) * r4[2 * e + 1]);
        }
    }
    const unsigned so = layer * 2 * MB + (2 * j) * 65536 + tt * 1024;
    const unsigned tk = (t & 0x13) | ((t & 4) << 1) | ((t & 8) >> 1);
    const unsigned vq = lane * 16, vk = (tk + 32 * h) * 16, vv = (t >> 4) * 65536 + h * 256 + (t & 15) * 16;
#pragma unroll
    for (int s = 0; s < 2; ++s) {
        st16<SC1>(u32x4{pk[4 * s], pk[4 * s + 1], pk[4 * s + 2], pk[4 * s + 3]}, c.rws, vk, OFF_K + so + s * 65536);
        st16<SC1>(u32x4{pv[4 * s], pv[4 * s + 1], pv[4 * s + 2], pv[4 * s + 3]}, c.rws, vv + s * 512, OFF_V + so);
        st16<SC1>(u32x4{pq[4 * s], pq[4 * s + 1], pq[4 * s + 2], pq[4 * s + 3]}, c.rws, vq, OFF_Q + so + s * 65536);
    }
    set_flag(c.ws, OFF_FQKV + layer * 8192 + j * 512 + (tt & 7) * 64 + (tt >> 3) * 8, c.tag, lane);
}

DI void phaseA_mfma(const Ctx& c0) {
    const Ctx c = fresh(c0); const unsigned lane = lane_id();
    const unsigned w = c.w, t = lane & 31, h = lane >> 5;
    qkv_w_chunk<0>(c, 0, w, lane); qkv_w_chunk<1>(c, 0, w, lane); qkv_w_chunk<2>(c, 0, w, lane); qkv_w_chunk<3>(c, 0, w, lane);
    unsigned wl[2];
    wl[0] = (8 * h + ((lane & 15) >> 2)) * 64 + ((lane >> 4) & 1) * 32 + (lane & 3) * 8; wl[1] = wl[0] + 49152;
    PIN(wl[0]); PIN(wl[1]);
    unsigned xa[4];
#pragma unroll
    for (int cl = 0; cl < 4; ++cl) { xa[cl] = L_XR + (32 * w + t) * 128 + (((2 * cl + h) ^ ((t >> 1) & 7)) * 16); PIN(xa[cl]); }
    const unsigned one = ((lane & 15) == ((lane >> 4) & 1)) ? 0x3f803f80u : 0u;
    const u32x4 sel = {one, one, one, one};
    f32x16 aq = {}, ak = {}, av = {};
    f32x4 cs0 = {}, cs1 = {}, cs2 = {};
#pragma unroll
    for (int xc = 0; xc < 8; ++xc) {
        if (xc == 0) vmwait<18>(); else if (xc == 2) vmwait<12>(); else if (xc == 4) vmwait<6>(); else if (xc == 6) vmwait<0>();
        bar_lds();
#pragma unroll
        for (int cl = 0; cl < 4; ++cl) {
            const int kb = 4 * xc + cl;
            const u32x4 xf = lds_ld<u32x4>(xa[cl] + (xc % 3) * 16384);
            const u32x4 f0 = qkv_wfrag<true>(wl, kb, 0), f1 = qkv_wfrag<true>(wl, kb, 1), f2 = qkv_wfrag<true>(wl, kb, 2);
            aq = mfma32(f0, xf, aq); ak = mfma32(f1, xf, ak); av = mfma32(xf, f2, av);
            if (w == (unsigned)cl) { cs0 = mfma16(sel, f0, cs0); cs1 = mfma16(sel, f1, cs1); cs2 = mfma16(sel, f2, cs2); }
        }
    }
    if (lane < 16) {
        lds_add(L_CSQ + lane * 4, cs0[0]); lds_add(L_CSQ + (16 + lane) * 4, cs0[1]);
        lds_add(L_CSQ + (32 + lane) * 4, cs1[0]); lds_add(L_CSQ + (48 + lane) * 4, cs1[1]);
        lds_add(L_CSQ + (64 + lane) * 4, cs2[0]); lds_add(L_CSQ + (80 + lane) * 4, cs2[1]);
    }
    bar_lds();
    qkv_epilogue(c, 0, 8 * c.g + 4 * (c.r >> 4) + w, lane, aq, ak, av);
}

DI u32x4 gather_frag(rsrc_t r, unsigned vo, unsigned so, unsigned pitch) {
    unsigned e[8];
#pragma unroll
    for (int j = 0; j < 8; ++j) e[j] = (unsigned short)__builtin_amdgcn_raw_buffer_load_b16(r, vo, so + (8 * (j >> 2) + (j & 3)) * pitch, 0);
    return u32x4{e[0] | (e[1] << 16), e[2] | (e[3] << 16), e[4] | (e[5] << 16), e[6] | (e[7] << 16)};
}

DI void phaseA_help(const Ctx& c0, const void* wo, const void* wg, const void* wu, const void* wd) {
    const Ctx c = fresh(c0); const unsigned lane = lane_id();
    const unsigned hw = c.w - 4, t = lane & 31, h = lane >> 5;
    const unsigned tok0 = 256 * c.g + 128 * (c.r >> 4) + 32 * hw;
    const unsigned vo0 = (lane >> 3) * 1024 + (((lane & 7) ^ (lane >> 4)) * 16), vo1 = (lane >> 3) * 1024 + (((lane & 7) ^ ((lane >> 4) + 4)) * 16);
    auto xchunk = [&](int xc) {
#pragma unroll
        for (int p = 0; p < 4; ++p) dma<false, 0>(c.rx, (p & 1) ? vo1 : vo0, (tok0 + 8 * p) * 1024 + xc * 128, L_XR + (xc % 3) * 16384 + hw * 4096 + p * 1024);
    };
    xchunk(0); xchunk(1); xchunk(2);
    if (hw == 0) { lds_st<float>(L_CSQ + lane * 4, 0.f); lds_st<float>(L_CSQ + 256 + lane * 4, 0.f); lds_st<float>(L_CSP + lane * 4, 0.f); }
    unsigned xa[4];
#pragma unroll
    for (int cl = 0; cl < 4; ++cl) { xa[cl] = L_XR + (32 * hw + t) * 128 + (((2 * cl + h) ^ ((t >> 1) & 7)) * 16); PIN(xa[cl]); }
    float s0 = 0.f, s1 = 0.f, q0 = 0.f, q1 = 0.f;
#pragma unroll
    for (int xc = 0; xc < 8; ++xc) {
        if (xc == 0) vmwait<8>(); else if (xc < 7) vmwait<4>(); else vmwait<0>();
        bar_lds();
        if (xc >= 1 && xc <= 5) xchunk(xc + 2);
#pragma unroll
        for (int cl = 0; cl < 4; ++cl) {
            const u32x4 xf = lds_ld<u32x4>(xa[cl] + (xc % 3) * 16384);
#pragma unroll
            for (int e = 0; e < 4; ++e) { const float lo = bflo(xf[e]), hi = bfhi(xf[e]); s0 += lo; s1 += hi; q0 = fmaf(lo, lo, q0); q1 = fmaf(hi, hi, q1); }
        }
    }
    float mu, rs;
    finish_stats(s0 + s1, q0 + q1, mu, rs);
    if (lane < 32) { lds_st<float>(L_MU + (32 * hw + t) * 4, mu); lds_st<float>(L_RS + (32 * hw + t) * 4, rs); }
    bar_lds();
    if (c.b < 224) {
        const rsrc_t rg = mkrsrc(wg, DM * FF * 2), ru = mkrsrc(wu, DM * FF * 2), rd = mkrsrc(wd, FF * DM * 2);
        const unsigned fb = c.b >> 1, kb0 = 16 * (c.b & 1) + 4 * hw;
        const unsigned vg = (4 * h) * (FF * 2) + (t & 15) * 2 + ((t & 16) ? 0x80000000u : 0u), vu = vg ^ 0x80000000u;
        u32x4 fg[4], fd[2];
#pragma unroll
        for (int q = 0; q < 4; ++q) {
            const unsigned so = (kb0 + q) * 16 * (FF * 2) + fb * 32;
            fg[q] = gather_frag(rg, vg, so, FF * 2) | gather_frag(ru, vu, so, FF * 2);
        }
        const unsigned u0 = 2 * (c.b * 4 + hw), fp = u0 / 224, rem = u0 % 224, kbd = 56 * ((rem >> 1) & 1) + (rem >> 2);
#pragma unroll
        for (int f = 0; f < 2; ++f) fd[f] = gather_frag(rd, (4 * h) * (DM * 2) + t * 2, kbd * 16 * (DM * 2) + (2 * fp + f) * 64, DM * 2);
        float cs = 0.f;
#pragma unroll
        for (int q = 0; q < 4; ++q) {
            st16<SC1>(fg[q], c.rws, lane * 16, OFF_WG + (((fb / 7) * 32 + kb0 + q) * 7 + fb % 7) * 1024);
#pragma unroll
            for (int e = 0; e < 4; ++e) cs += bflo(fg[q][e]) + bfhi(fg[q][e]);
        }
#pragma unroll
        for (int f = 0; f < 2; ++f) st16<SC1>(fd[f], c.rws, lane * 16, OFF_WD + (u0 + f) * 1024);
        lds_add(L_CSP + t * 4, cs);
    } else {
        const rsrc_t ro = mkrsrc(wo, DM * DM * 2);
        const unsigned u0 = 4 * ((c.b - 224) * 4 + hw), fp = u0 >> 6, st = (u0 >> 2) & 15;
        u32x4 fo[4];
#pragma unroll
        for (int q = 0; q < 4; ++q) fo[q] = gather_frag(ro, (4 * h) * (DM * 2) + t * 2, (16 * (q >> 1) + st) * 16 * (DM * 2) + (2 * fp + (q & 1)) * 64, DM * 2);
#pragma unroll
        for (int q = 0; q < 4; ++q) st16<SC1>(fo[q], c.rws, lane * 16, OFF_WO + (u0 + q) * 1024);
    }
}

#define ATT_PROLOGUE \
    "s_nop 1\n\tds_read_b128 v[80:83], v96\n\tds_read_b128 v[84:87], v97\n\tds_read_b128 v[88:91], v97 offset:512\n\tv_mov_b32 v48, 0\n\tv_mov_b32 v49, 0\n\t" \
    "v_mov_b32 v50, 0\n\tv_mov_b32 v51, 0\n\tv_mov_b32 v52, 0\n\tv_mov_b32 v53, 0\n\tv_mov_b32 v54, 0\n\tv_mov_b32 v55, 0\n\tv_mov_b32 v56, 0\n\tv_mov_b32 v57, 0\n\t" \
    "v_mov_b32 v58, 0\n\tv_mov_b32 v59, 0\n\tv_mov_b32 v60, 0\n\tv_mov_b32 v61, 0\n\tv_mov_b32 v62, 0\n\tv_mov_b32 v63, 0\n\ts_waitcnt lgkmcnt(0)\n\t" \
    "v_mfma_f32_32x32x16_bf16 v[16:31], v[80:83], v[92:95], v[64:79]\n\tds_read_b128 v[80:83], v96 offset:1024\n\ts_nop 15\n\tv_exp_f32 v16, v16\n\tv_exp_f32 v17, v17\n\t" \
    "s_waitcnt lgkmcnt(0)\n\t"
#define ATT_LOOP \
    "s_nop 1\n\t1:\n\tv_exp_f32 v18, v18\n\tv_exp_f32 v19, v19\n\tv_exp_f32 v20, v20\n\tv_exp_f32 v21, v21\n\tv_exp_f32 v22, v22\n\ts_waitcnt lgkmcnt(2)\n\t" \
    "v_mfma_f32_32x32x16_bf16 v[32:47], v[80:83], v[92:95], v[64:79]\n\tds_read_b128 v[80:83], v96 offset:2048\n\tv_exp_f32 v23, v23\n\tv_exp_f32 v24, v24\n\t" \
    "v_exp_f32 v25, v25\n\tv_exp_f32 v26, v26\n\tv_exp_f32 v27, v27\n\ts_waitcnt lgkmcnt(2)\n\tv_smfmac_f32_32x32x32_bf16 v[48:63], v[84:87], v[16:23], v99\n\t" \
    "ds_read_b128 v[84:87], v97 offset:1024\n\tv_exp_f32 v28, v28\n\tv_exp_f32 v29, v29\n\tv_exp_f32 v30, v30\n\tv_exp_f32 v31, v31\n\tv_exp_f32 v32, v32\n\t" \
    "v_exp_f32 v33, v33\n\ts_waitcnt lgkmcnt(2)\n\tv_smfmac_f32_32x32x32_bf16 v[48:63], v[88:91], v[24:31], v99\n\tds_read_b128 v[88:91], v97 offset:1536\n\t" \
    "v_exp_f32 v34, v34\n\tv_exp_f32 v35, v35\n\tv_exp_f32 v36, v36\n\tv_exp_f32 v37, v37\n\tv_exp_f32 v38, v38\n\ts_waitcnt lgkmcnt(2)\n\t" \
    "v_mfma_f32_32x32x16_bf16 v[16:31], v[80:83], v[92:95], v[64:79]\n\tds_read_b128 v[80:83], v96 offset:3072\n\tv_exp_f32 v39, v39\n\tv_exp_f32 v40, v40\n\t" \
    "v_exp_f32 v41, v41\n\tv_exp_f32 v42, v42\n\tv_exp_f32 v43, v43\n\ts_waitcnt lgkmcnt(2)\n\tv_smfmac_f32_32x32x32_bf16 v[48:63], v[84:87], v[32:39], v99\n\t" \
    "ds_read_b128 v[84:87], v97 offset:2048\n\tv_exp_f32 v44, v44\n\tv_exp_f32 v45, v45\n\tv_exp_f32 v46, v46\n\tv_exp_f32 v47, v47\n\tv_exp_f32 v16, v16\n\t" \
    "v_exp_f32 v17, v17\n\ts_waitcnt lgkmcnt(2)\n\tv_smfmac_f32_32x32x32_bf16 v[48:63], v[88:91], v[40:47], v99\n\tds_read_b128 v[88:91], v97 offset:2560\n\t" \
    "v_exp_f32 v18, v18\n\tv_exp_f32 v19, v19\n\tv_exp_f32 v20, v20\n\tv_exp_f32 v21, v21\n\tv_exp_f32 v22, v22\n\ts_waitcnt lgkmcnt(2)\n\t" \
    "v_mfma_f32_32x32x16_bf16 v[32:47], v[80:83], v[92:95], v[64:79]\n\tds_read_b128 v[80:83], v96 offset:4096\n\tv_exp_f32 v23, v23\n\tv_exp_f32 v24, v24\n\t" \
    "v_exp_f32 v25, v25\n\tv_exp_f32 v26, v26\n\tv_exp_f32 v27, v27\n\ts_waitcnt lgkmcnt(2)\n\tv_smfmac_f32_32x32x32_bf16 v[48:63], v[84:87], v[16:23], v99\n\t" \
    "ds_read_b128 v[84:87], v97 offset:3072\n\tv_exp_f32 v28, v28\n\tv_exp_f32 v29, v29\n\tv_exp_f32 v30, v30\n\tv_exp_f32 v31, v31\n\tv_exp_f32 v32, v32\n\t" \
    "v_exp_f32 v33, v33\n\ts_waitcnt lgkmcnt(2)\n\tv_smfmac_f32_32x32x32_bf16 v[48:63], v[88:91], v[24:31], v99\n\tds_read_b128 v[88:91], v97 offset:3584\n\t" \
    "v_exp_f32 v34, v34\n\tv_exp_f32 v35, v35\n\tv_exp_f32 v36, v36\n\tv_exp_f32 v37, v37\n\tv_exp_f32 v38, v38\n\ts_waitcnt lgkmcnt(2)\n\t" \
    "v_mfma_f32_32x32x16_bf16 v[16:31], v[80:83], v[92:95], v[64:79]\n\tds_read_b128 v[80:83], v96 offset:5120\n\tv_exp_f32 v39, v39\n\tv_exp_f32 v40, v40\n\t" \
    "v_exp_f32 v41, v41\n\tv_exp_f32 v42, v42\n\tv_exp_f32 v43, v43\n\ts_waitcnt lgkmcnt(2)\n\tv_smfmac_f32_32x32x32_bf16 v[48:63], v[84:87], v[32:39], v99\n\t" \
    "ds_read_b128 v[84:87], v97 offset:4096\n\tv_exp_f32 v44, v44\n\tv_exp_f32 v45, v45\n\tv_exp_f32 v46, v46\n\tv_exp_f32 v47, v47\n\tv_exp_f32 v16, v16\n\t" \
    "v_exp_f32 v17, v17\n\ts_waitcnt lgkmcnt(2)\n\tv_smfmac_f32_32x32x32_bf16 v[48:63], v[88:91], v[40:47], v99\n\tds_read_b128 v[88:91], v97 offset:4608\n\t" \
    "v_exp_f32 v18, v18\n\tv_exp_f32 v19, v19\n\tv_exp_f32 v20, v20\n\tv_exp_f32 v21, v21\n\tv_exp_f32 v22, v22\n\ts_waitcnt lgkmcnt(2)\n\t" \
    "v_mfma_f32_32x32x16_bf16 v[32:47], v[80:83], v[92:95], v[64:79]\n\tds_read_b128 v[80:83], v96 offset:6144\n\tv_exp_f32 v23, v23\n\tv_exp_f32 v24, v24\n\t" \
    "v_exp_f32 v25, v25\n\tv_exp_f32 v26, v26\n\tv_exp_f32 v27, v27\n\ts_waitcnt lgkmcnt(2)\n\tv_smfmac_f32_32x32x32_bf16 v[48:63], v[84:87], v[16:23], v99\n\t" \
    "ds_read_b128 v[84:87], v97 offset:5120\n\tv_exp_f32 v28, v28\n\tv_exp_f32 v29, v29\n\tv_exp_f32 v30, v30\n\tv_exp_f32 v31, v31\n\tv_exp_f32 v32, v32\n\t" \
    "v_exp_f32 v33, v33\n\ts_waitcnt lgkmcnt(2)\n\tv_smfmac_f32_32x32x32_bf16 v[48:63], v[88:91], v[24:31], v99\n\tds_read_b128 v[88:91], v97 offset:5632\n\t" \
    "v_exp_f32 v34, v34\n\tv_exp_f32 v35, v35\n\tv_exp_f32 v36, v36\n\tv_exp_f32 v37, v37\n\tv_exp_f32 v38, v38\n\ts_waitcnt lgkmcnt(2)\n\t" \
    "v_mfma_f32_32x32x16_bf16 v[16:31], v[80:83], v[92:95], v[64:79]\n\tds_read_b128 v[80:83], v96 offset:7168\n\tv_exp_f32 v39, v39\n\tv_exp_f32 v40, v40\n\t" \
    "v_exp_f32 v41, v41\n\tv_exp_f32 v42, v42\n\tv_exp_f32 v43, v43\n\ts_waitcnt lgkmcnt(2)\n\tv_smfmac_f32_32x32x32_bf16 v[48:63], v[84:87], v[32:39], v99\n\t" \
    "ds_read_b128 v[84:87], v97 offset:6144\n\tv_exp_f32 v44, v44\n\tv_exp_f32 v45, v45\n\tv_exp_f32 v46, v46\n\tv_exp_f32 v47, v47\n\tv_exp_f32 v16, v16\n\t" \
    "v_exp_f32 v17, v17\n\ts_waitcnt lgkmcnt(2)\n\tv_smfmac_f32_32x32x32_bf16 v[48:63], v[88:91], v[40:47], v99\n\tds_read_b128 v[88:91], v97 offset:6656\n\t" \
    "v_exp_f32 v18, v18\n\tv_exp_f32 v19, v19\n\tv_exp_f32 v20, v20\n\tv_exp_f32 v21, v21\n\tv_exp_f32 v22, v22\n\ts_waitcnt lgkmcnt(2)\n\t" \
    "v_mfma_f32_32x32x16_bf16 v[32:47], v[80:83], v[92:95], v[64:79]\n\tds_read_b128 v[80:83], v96 offset:8192\n\tv_exp_f32 v23, v23\n\tv_exp_f32 v24, v24\n\t" \
    "v_exp_f32 v25, v25\n\tv_exp_f32 v26, v26\n\tv_exp_f32 v27, v27\n\ts_waitcnt lgkmcnt(2)\n\tv_smfmac_f32_32x32x32_bf16 v[48:63], v[84:87], v[16:23], v99\n\t" \
    "ds_read_b128 v[84:87], v97 offset:7168\n\tv_exp_f32 v28, v28\n\tv_exp_f32 v29, v29\n\tv_exp_f32 v30, v30\n\tv_exp_f32 v31, v31\n\tv_exp_f32 v32, v32\n\t" \
    "v_exp_f32 v33, v33\n\ts_waitcnt lgkmcnt(2)\n\tv_smfmac_f32_32x32x32_bf16 v[48:63], v[88:91], v[24:31], v99\n\tds_read_b128 v[88:91], v97 offset:7680\n\t" \
    "v_exp_f32 v34, v34\n\tv_exp_f32 v35, v35\n\tv_exp_f32 v36, v36\n\tv_exp_f32 v37, v37\n\tv_exp_f32 v38, v38\n\ts_waitcnt lgkmcnt(2)\n\t" \
    "v_mfma_f32_32x32x16_bf16 v[16:31], v[80:83], v[92:95], v[64:79]\n\tds_read_b128 v[80:83], v96 offset:9216\n\tv_exp_f32 v39, v39\n\tv_exp_f32 v40, v40\n\t" \
    "v_exp_f32 v41, v41\n\tv_exp_f32 v42, v42\n\tv_exp_f32 v43, v43\n\ts_waitcnt lgkmcnt(2)\n\tv_smfmac_f32_32x32x32_bf16 v[48:63], v[84:87], v[32:39], v99\n\t" \
    "v_add_u32 v97, v97, v98\n\tds_read_b128 v[84:87], v97 offset:0\n\tv_exp_f32 v44, v44\n\tv_exp_f32 v45, v45\n\tv_exp_f32 v46, v46\n\tv_exp_f32 v47, v47\n\t" \
    "v_exp_f32 v16, v16\n\tv_exp_f32 v17, v17\n\ts_waitcnt lgkmcnt(2)\n\tv_smfmac_f32_32x32x32_bf16 v[48:63], v[88:91], v[40:47], v99\n\t" \
    "ds_read_b128 v[88:91], v97 offset:512\n\tv_add_u32 v96, 0x2000, v96\n\ts_sub_u32 %[cnt], %[cnt], 1\n\ts_cmp_lg_u32 %[cnt], 0\n\ts_cbranch_scc1 1b\n\t" \
    "s_waitcnt lgkmcnt(0)\n\ts_nop 15\n\t"

DI void attention(const Ctx& c0, unsigned layer) {
    const Ctx c = fresh(c0); const unsigned lane = lane_id();
    const unsigned w = c.w, head = 4 * c.g + (c.r & 3), tt = 8 * (c.r >> 2) + w;
    const unsigned t = lane & 31, h = lane >> 5;
    unsigned ones = 0x3f803f80u;
    PIN(ones);
    lds_st<u32x4>(L_EX + (w * 64 + lane) * 16, u32x4{ones, ones, ones, ones});
    wait_flags(c.ws, OFF_FQKV + layer * 8192 + (head >> 1) * 512 + w * 64 + (lane & 7) * 8, c.tag);
    const unsigned hso = layer * 2 * MB + head * 65536;
    u32x4 qf;
    asm volatile("buffer_load_dwordx4 %0, %1, %2, %3 offen sc1" : "=v"(qf) : "v"(lane * 16), "s"(c.rws), "s"(OFF_Q + hso + tt * 1024) : "memory");
#pragma unroll
    for (int i = 0; i < 8; ++i) {
        dma<true, 0>(c.rws, lane * 16, OFF_K + hso + (w + 8 * i) * 1024, (w + 8 * i) * 1024);
        dma<true, 0>(c.rws, lane * 16, OFF_V + hso + (w + 8 * i) * 1024, L_V + (w + 8 * i) * 1024);
    }
    asm volatile("s_waitcnt vmcnt(12)" : "+v"(qf)::"memory");
    bar_lds();
    const bool isv = t < 16;
    const unsigned ka0 = lane * 16;
    const unsigned va0 = isv ? L_V + h * 256 + t * 16 : L_EX + ((t >= 20 && t < 28) ? 64u : 0u);
    const unsigned vstep = isv ? 8192u : 0u;
    int idx = 0xDDDD;
    f32x16 cinit, o, sa, sb;
    u32x4 kf, v0, v1;
    {
        float z = 0.f;
        asm volatile("" : "+v"(z));
#pragma unroll
        for (int e = 0; e < 16; ++e) cinit[e] = z;
    }
    unsigned first = 1;
#pragma unroll 1
    while (true) {
        unsigned ka = ka0, va = va0;
        asm volatile(ATT_PROLOGUE
                     : "={v[16:31]}"(sa), "={v[48:63]}"(o), "={v[80:83]}"(kf), "={v[84:87]}"(v0), "={v[88:91]}"(v1)
                     : "{v[64:79]}"(cinit), "{v[92:95]}"(qf), "{v96}"(ka), "{v97}"(va) : "memory");
#pragma unroll 1
        for (unsigned part = 0; part < 3; ++part) {
            unsigned cnt = part == 0 ? 1 : part == 1 ? 2 : 5;
            asm volatile(ATT_LOOP
                         : "+{v[16:31]}"(sa), "={v[32:47]}"(sb), "+{v[48:63]}"(o), "+{v[80:83]}"(kf), "+{v[84:87]}"(v0), "+{v[88:91]}"(v1), "+{v96}"(ka), "+{v97}"(va), [cnt] "+s"(cnt)
                         : "{v[64:79]}"(cinit), "{v[92:95]}"(qf), "{v98}"(vstep), "{v99}"(idx) : "scc", "memory");
            if (part == 0 && first) {
                vmwait<0>();
                bar();
                if (layer == 0 && w == 7) {
                    if (c.b < 224 && lane < 32) __builtin_amdgcn_raw_buffer_store_b32(bc<unsigned>(lds_ld<float>(L_CSP + lane * 4)), c.rws, lane * 4, OFF_CSG + ((c.b & 1) * 112 + (c.b >> 1)) * 128, SC1);
                    set_flag(c.ws, OFF_FW + c.b * 8, c.tag, lane);
                }
            }
            if (part == 1 && first && layer == 0 && w >= 4) {
#pragma unroll 1
                for (int q = 0; q < 4; ++q) wait_flags(c.ws, OFF_FW + q * 512 + lane * 8, c.tag);
                if (w == 4 && lane < 56) {
                    const unsigned so = OFF_CSG + (c.r & 15) * 896;
                    lds_st<f32x4>(L_CSG + lane * 16, bc<f32x4>(ld16<SC1>(c.rws, lane * 16, so)) + bc<f32x4>(ld16<SC1>(c.rws, lane * 16, so + 112 * 128)));
                }
            }
        }
        const float l = o[8];
        const float mag = fabsf(o[0]) + fabsf(o[1]) + fabsf(o[2]) + fabsf(o[3]) + fabsf(o[4]) + fabsf(o[5]) + fabsf(o[6]) + fabsf(o[7]) + l;
        const bool ok = (l >= 9.313225746e-10f) && (mag < __builtin_inff());
        if (!first || __all(ok)) break;
        float m = -__builtin_inff();
#pragma unroll 1
        for (int i = 0; i < 64; ++i) {
            const f32x16 s = mfma32(lds_ld<u32x4>(ka0 + i * 1024), qf, f32x16{});
#pragma unroll
            for (int e = 0; e < 16; ++e) m = fmaxf(m, s[e]);
        }
        m = -swap32_max(m);
#pragma unroll
        for (int e = 0; e < 16; ++e) cinit[e] = m;
        first = 0;
        asm volatile("" : "+s"(first));
    }
    const float inv = 1.0f / o[8];
    st16<SC1>(u32x4{pk2(o[0] * inv, o[1] * inv), pk2(o[2] * inv, o[3] * inv), pk2(o[4] * inv, o[5] * inv), pk2(o[6] * inv, o[7] * inv)},
              c.rws, lane * 16, OFF_O + layer * 2 * MB + (tt * 32 + head) * 1024);
    bar_lds();
    set_flag(c.ws, OFF_FO + layer * 16384 + tt * 256 + head * 8, c.tag, lane);
}

constexpr int chunk_pieces(int n) { return n > 16 ? 0 : (n >= 2 && n <= 9) ? 7 : 8; }
template <int N> DI void issue_chunk(const Ctx& c, unsigned lane) {
    if constexpr (N <= 16) {
        constexpr int np = chunk_pieces(N);
        const unsigned hw = c.w - 4;
        const unsigned src = (N < 2 ? OFF_WO + (c.r & 7) * 65536 + N * 32768 : N < 10 ? OFF_WG + (c.r & 15) * 229376 + (N - 2) * 28672 : OFF_WD + (c.r & 7) * 229376 + (N - 10) * 32768) + hw * (np * 1024);
        const unsigned dst = (N & 3) * L_SLOT + hw * (np * 1024), vo = lane * 16;
        dma<true, 0>(c.rws, vo, src, dst); dma<true, 1024>(c.rws, vo, src, dst); dma<true, 2048>(c.rws, vo, src, dst); dma<true, 3072>(c.rws, vo, src, dst);
        dma<true, 0>(c.rws, vo, src + 4096, dst + 4096); dma<true, 1024>(c.rws, vo, src + 4096, dst + 4096); dma<true, 2048>(c.rws, vo, src + 4096, dst + 4096);
        if constexpr (np == 8) dma<true, 3072>(c.rws, vo, src + 4096, dst + 4096);
    }
}
template <int N> DI void stream_steps(const Ctx& c, unsigned lane) {
    if constexpr (N <= 17) {
        vmwait<chunk_pieces(N + 2)>();
        bar();
        issue_chunk<N + 3>(c, lane);
        stream_steps<N + 1>(c, lane);
    }
}
DI void chain_help(const Ctx& c0, unsigned layer) {
    const Ctx c = fresh(c0); const unsigned lane = lane_id();
    issue_chunk<0>(c, lane); issue_chunk<1>(c, lane); issue_chunk<2>(c, lane); issue_chunk<3>(c, lane);
    vmwait<14>();
    bar();
    stream_steps<1>(c, lane);
    if (layer == 0) {
        const unsigned hw = c.w - 4;
        qkv_w_chunk<0>(c, L_SLOT, hw, lane); qkv_w_chunk<1>(c, L_SLOT, hw, lane); qkv_w_chunk<2>(c, L_SLOT, hw, lane); qkv_w_chunk<3>(c, L_SLOT, hw, lane);
        vmwait<0>();
        bar();
        bar();
    }
}

template <int MODE> DI void proj(const Ctx& c0, unsigned layer, void* out) {
    const Ctx c = fresh(c0); const unsigned lane = lane_id();
    constexpr int KS = MODE ? 56 : 16, N0 = MODE ? 10 : 0, TR = MODE ? 28 : 16, R = 5, P = 4;
    const unsigned w = c.w, kh = w >> 1, tt = 8 * c.g + 2 * (c.r >> 3) + (w & 1), ft = 2 * (c.r & 7) + kh;
    const unsigned t = lane & 31, h = lane >> 5, vo = lane * 16;
    unsigned res[8];
    if (MODE == 0 && layer == 0) {
#pragma unroll
        for (int q = 0; q < 4; ++q) { const u32x2 v = ld8<0>(c.rx, t * 1024 + h * 8 + q * 16, tt * 32768 + ft * 64); res[2 * q] = v.x; res[2 * q + 1] = v.y; }
    } else {
        const unsigned ro = (MODE ? OFF_X1 + layer * 2 * MB : OFF_X2) + (tt * 32 + 2 * ft) * 1024;
#pragma unroll
        for (int s = 0; s < 2; ++s) { const u32x4 v = ldfrag(c.rws, vo, ro, s); res[4 * s] = v.x; res[4 * s + 1] = v.y; res[4 * s + 2] = v.z; res[4 * s + 3] = v.w; }
    }
    if (MODE) wait_flags(c.ws, OFF_FH + layer * 8192 + tt * 128 + kh * 64 + (lane & 7) * 8, c.tag);
    else wait_flags(c.ws, OFF_FO + layer * 16384 + tt * 256 + kh * 128 + (lane & 15) * 8, c.tag);
    const unsigned tso = MODE ? OFF_H + layer * 7 * MB + (tt * 112 + 56 * kh) * 1024 : OFF_O + layer * 2 * MB + (tt * 32 + 16 * kh) * 1024;
    u32x4 tok[TR], wf[R];
#pragma unroll
    for (int s = 0; s < TR; ++s) tok[s] = ldfrag(c.rws, vo, tso, s);
    if (MODE == 0) bar();
    unsigned wa[4] = {vo + kh * 3072, vo + kh * 1024 + 1024, vo + kh * 3072 + 2 * L_SLOT, vo + kh * 1024 + 1024 + 2 * L_SLOT};
    PIN(wa[0]); PIN(wa[1]); PIN(wa[2]); PIN(wa[3]);
    auto wread = [&](int n) { const int s = n >> 1, sl = (N0 + (s >> 3)) & 3; return lds_ld<u32x4>(wa[(n & 1) + (sl & 2)] + (sl & 1) * L_SLOT + (s & 7) * 4096); };
#pragma unroll
    for (int n = 0; n < P; ++n) wf[n] = wread(n);
    f32x16 a0 = {}, a1 = {};
#pragma unroll
    for (int n = 0; n < 2 * KS; ++n) {
        const int s = n >> 1;
        if (n + P < 2 * KS) wf[(n + P) % R] = wread(n + P);
        if (n & 1) { a1 = mfma32(wf[n % R], tok[s % TR], a1); PIN(a1); } else { a0 = mfma32(wf[n % R], tok[s % TR], a0); PIN(a0); }
        if ((n & 1) && s + TR < KS) tok[s % TR] = ldfrag(c.rws, vo, tso, s + TR);
        SB();
        if ((n & 15) == 15 && n + 1 < 2 * KS) bar();
    }
#pragma unroll
    for (int q = 0; q < 4; ++q) lds_st<f32x4>(L_EX + w * 4096 + q * 1024 + vo, f32x4{a1[4 * q], a1[4 * q + 1], a1[4 * q + 2], a1[4 * q + 3]});
    bar_lds();
    unsigned p[8];
    float s1 = 0.f, s2 = 0.f;
#pragma unroll
    for (int q = 0; q < 4; ++q) {
        const f32x4 e = lds_ld<f32x4>(L_EX + (w ^ 2) * 4096 + q * 1024 + vo);
        const float y0 = a0[4 * q] + e[0] + bflo(res[2 * q]), y1 = a0[4 * q + 1] + e[1] + bfhi(res[2 * q]);
        const float y2 = a0[4 * q + 2] + e[2] + bflo(res[2 * q + 1]), y3 = a0[4 * q + 3] + e[3] + bfhi(res[2 * q + 1]);
        p[2 * q] = pk2(y0, y1); p[2 * q + 1] = pk2(y2, y3);
        s1 += (y0 + y1) + (y2 + y3);
        s2 = fmaf(y0, y0, fmaf(y1, y1, fmaf(y2, y2, fmaf(y3, y3, s2))));
    }
    if (MODE == 1 && layer == 1) {
        const rsrc_t ro = mkrsrc(out, SEQ * DM * 2);
#pragma unroll
        for (int s = 0; s < 2; ++s) {
            const auto x = __builtin_amdgcn_permlane32_swap(p[4 * s], p[4 * s + 2], false, false);
            const auto y = __builtin_amdgcn_permlane32_swap(p[4 * s + 1], p[4 * s + 3], false, false);
            st16<0>(u32x4{x[0], y[0], x[1], y[1]}, ro, t * 1024 + h * 16, tt * 32768 + ft * 64 + s * 32);
        }
        return;
    }
    const unsigned xo = (MODE ? OFF_X2 : OFF_X1 + layer * 2 * MB) + (tt * 32 + 2 * ft) * 1024;
    st16<SC1>(u32x4{p[0], p[1], p[2], p[3]}, c.rws, vo, xo);
    st16<SC1>(u32x4{p[4], p[5], p[6], p[7]}, c.rws, vo + 1024, xo);
    s1 = swap32_add(s1); s2 = swap32_add(s2);
    if (lane < 32) st8<SC1>(u32x2{bc<unsigned>(s1), bc<unsigned>(s2)}, c.rws, lane * 8, (MODE ? OFF_ST2 : OFF_ST1 + layer * 262144) + tt * 4096 + ft * 256);
    set_flag(c.ws, (MODE ? OFF_FX2 : OFF_FX1 + layer * 8192) + tt * 128 + ft * 8, c.tag, lane);
}

DI void gateup(const Ctx& c0, unsigned layer) {
    const Ctx c = fresh(c0); const unsigned lane = lane_id();
    constexpr int TR = 8, R = 6, P = 5;
    const unsigned w = c.w, cj = c.r & 15, tt = 8 * c.g + 4 * (c.r >> 4) + w, h = lane >> 5, vo = lane * 16;
    wait_flags(c.ws, OFF_FX1 + layer * 8192 + tt * 128 + (lane & 15) * 8, c.tag);
    const unsigned tso = OFF_X1 + layer * 2 * MB + tt * 32768;
    u32x4 tok[TR], wf[R];
    u32x2 sp[8];
#pragma unroll
    for (int s = 0; s < TR; ++s) tok[s] = ldfrag(c.rws, vo, tso, s);
    load_stats(c, OFF_ST1 + layer * 262144, tt, lane, sp);
    unsigned vh = vo + 2 * L_SLOT;
    PIN(vh);
    auto wread = [&](int n) { const int sl = (2 + n / 28) & 3; return lds_ld<u32x4>((sl & 2 ? vh : vo) + (sl & 1) * L_SLOT + (n % 28) * 1024); };
#pragma unroll
    for (int n = 0; n < P; ++n) wf[n] = wread(n);
    f32x16 acc[7] = {};
    float mu, rs;
    static_for<0, 224>([&](auto ic) __attribute__((always_inline)) {
        constexpr int n = decltype(ic)::v, k = n / 7, i = n % 7;
        if constexpr (n + P < 224) wf[(n + P) % R] = wread(n + P);
        acc[i] = mfma32(wf[n % R], tok[k % TR], acc[i]); PIN(acc[i]);
        if constexpr (i == 6 && k + TR < 32) tok[k % TR] = ldfrag(c.rws, vo, tso, k + TR);
        if constexpr (n == 20) { reduce_stats(sp, mu, rs); asm volatile("" : "+v"(mu), "+v"(rs)); }
        SB();
        if constexpr (n % 28 == 27) bar();
    });
    const float r2 = rs * rs;
    const unsigned ho = OFF_H + layer * 7 * MB + (tt * 112 + 7 * cj) * 1024;
#pragma unroll
    for (int i = 0; i < 7; ++i) {
        unsigned p[4];
#pragma unroll
        for (int q = 0; q < 2; ++q) {
            const f32x4 cg = lds_ld<f32x4>(L_CSG + (i * 32 + 8 * q + 4 * h) * 4), cu = lds_ld<f32x4>(L_CSG + (i * 32 + 16 + 8 * q + 4 * h) * 4);
            float y[4];
#pragma unroll
            for (int e = 0; e < 4; ++e) y[e] = (acc[i][4 * q + e] - mu * cg[e]) * r2 * (acc[i][8 + 4 * q + e] - mu * cu[e]);
            p[2 * q] = pk2(y[0], y[1]); p[2 * q + 1] = pk2(y[2], y[3]);
        }
        stfrag(u32x4{p[0], p[1], p[2], p[3]}, c.rws, vo, ho, i);
    }
    set_flag(c.ws, OFF_FH + layer * 8192 + tt * 128 + cj * 8, c.tag, lane);
}

DI void phaseF(const Ctx& c0) {
    const Ctx c = fresh(c0); const unsigned lane = lane_id();
    const unsigned w = c.w, tt = 8 * c.g + 4 * (c.r >> 4) + w, t = lane & 31, h = lane >> 5, vo = lane * 16;
    wait_flags(c.ws, OFF_FX2 + tt * 128 + (lane & 15) * 8, c.tag);
    u32x4 tok[32];
    u32x2 sp[8];
#pragma unroll
    for (int s = 0; s < 8; ++s) tok[s] = ldfrag(c.rws, vo, OFF_X2 + tt * 32768, s);
    load_stats(c, OFF_ST2, tt, lane, sp);
#pragma unroll
    for (int s = 8; s < 32; ++s) tok[s] = ldfrag(c.rws, vo, OFF_X2 + tt * 32768, s);
    bar();
    unsigned wl[2];
    wl[0] = L_SLOT + (4 * h + ((lane & 15) >> 2)) * 64 + ((lane >> 4) & 1) * 32 + (lane & 3) * 8; wl[1] = wl[0] + 49152;
    PIN(wl[0]); PIN(wl[1]);
    constexpr int R = 5, P = 4;
    u32x4 wf[R];
#pragma unroll
    for (int n = 0; n < P; ++n) wf[n] = qkv_wfrag<false>(wl, n / 3, n % 3);
    f32x16 aq = {}, ak = {}, av = {};
#pragma unroll
    for (int n = 0; n < 96; ++n) {
        const int k = n / 3, i = n % 3;
        if (n + P < 96) wf[(n + P) % R] = qkv_wfrag<false>(wl, (n + P) / 3, (n + P) % 3);
        if (i == 0) { aq = mfma32(wf[n % R], tok[k], aq); PIN(aq); }
        else if (i == 1) { ak = mfma32(wf[n % R], tok[k], ak); PIN(ak); }
        else { av = mfma32(tok[k], wf[n % R], av); PIN(av); }
        if (n == 30) {
            float mu, rs;
            reduce_stats(sp, mu, rs);
            if (lane < 32) { lds_st<float>(L_MU + (32 * w + t) * 4, mu); lds_st<float>(L_RS + (32 * w + t) * 4, rs); }
        }
        SB();
    }
    bar_lds();
    qkv_epilogue(c, 1, tt, lane, aq, ak, av);
}

__global__ void __launch_bounds__(512, 2) fused_layers(const void* x, const void* wqkv, const void* wo, const void* wg, const void* wu, const void* wd, void* out, char* ws) {
    __shared__ __attribute__((aligned(16))) char lds[LDS_BYTES];
    asm volatile("" ::"v"(lds) : "memory");
    Ctx c;
    c.ws = ws; c.b = blockIdx.x; c.g = c.b & 7; c.r = c.b >> 3; c.w = rfl(threadIdx.x >> 6);
    c.rws = mkrsrc(ws, WS_USED); c.rx = mkrsrc(x, SEQ * DM * 2); c.rwqkv = mkrsrc(wqkv, DM * 3 * DM * 2);
    const u64 e = __hip_atomic_load((gu64*)(ws + OFF_EPOCH), RLX_AGENT) + 1;
    STAMP(0);
    if (c.w < 4) {
        c.tag = ((u64)rfl((unsigned)(e >> 32)) << 32) | rfl((unsigned)e);
        phaseA_mfma(c);
    } else {
        c.tag = ((u64)rfl((unsigned)(e >> 32)) << 32) | rfl((unsigned)e);
        phaseA_help(c, wo, wg, wu, wd);
    }
    STAMP(1);
#pragma unroll 1
    for (unsigned layer = 0; layer < 2; ++layer) {
        attention(c, layer);
        STAMP(2 + layer * 8);
        if (c.w < 4) {
            proj<0>(c, layer, out);
            STAMP(3 + layer * 8);
            gateup(c, layer);
            STAMP(4 + layer * 8);
            proj<1>(c, layer, out);
            STAMP(5 + layer * 8);
            if (layer == 0) phaseF(c);
            STAMP(6 + layer * 8);
        } else {
            chain_help(c, layer);
        }
    }
    if (c.b == 0 && c.w == 0 && lane_id() == 0) __hip_atomic_store((gu64*)(ws + OFF_EPOCH), c.tag, RLX_AGENT);
}

}

extern "C" void kernel_launch(void* const* d_in, const int* in_sizes, int n_in, void* d_out, int out_size, void* d_ws, size_t ws_size, hipStream_t stream) {
    (void)in_sizes; (void)n_in; (void)out_size; (void)ws_size;
    fused_layers<<<256, 512, 0, stream>>>(d_in[0], d_in[1], d_in[2], d_in[4], d_in[5], d_in[6], d_out, (char*)d_ws);
}
```

```cpp
#include <hip/hip_runtime.h>
#include <hip/hip_bf16.h>
#include <stdint.h>

#pragma clang diagnostic ignored "-Winline-asm"

typedef __attribute__((ext_vector_type(2))) unsigned u32x2;
typedef __attribute__((ext_vector_type(4))) unsigned u32x4;
typedef __attribute__((ext_vector_type(8))) unsigned u32x8;
typedef __attribute__((ext_vector_type(32))) unsigned u32x32;
typedef __attribute__((ext_vector_type(4))) float f32x4;
typedef __attribute__((ext_vector_type(8))) float f32x8;
typedef __attribute__((ext_vector_type(16))) float f32x16;
typedef __attribute__((ext_vector_type(4))) short s16x4;
typedef __bf16 bf16x2 __attribute__((ext_vector_type(2)));
typedef __bf16 bf16x8 __attribute__((ext_vector_type(8)));
typedef __bf16 bf16x16 __attribute__((ext_vector_type(16)));
typedef __amdgpu_buffer_rsrc_t rsrc_t;
typedef unsigned long long u64;
typedef __attribute__((address_space(1))) u64 gu64;
#define LAS __attribute__((address_space(3)))
#define DI __device__ __forceinline__
#define AI __attribute__((always_inline))
#define SB() __builtin_amdgcn_sched_barrier(0)
#define PIN(x) asm volatile("" : "+v"(x))
#define RLX_AGENT __ATOMIC_RELAXED, __HIP_MEMORY_SCOPE_AGENT

namespace {

constexpr int SEQ = 2048, DM = 512, FF = 1792;
constexpr int SC1 = 16;
constexpr unsigned ONES = 0x3f803f80u;

constexpr unsigned MB = 1u << 20;
constexpr unsigned OFF_EPOCH = 0;
constexpr unsigned OFF_FQKV = 4096;
constexpr unsigned OFF_FO = OFF_FQKV + 2 * 16 * 64 * 8;
constexpr unsigned OFF_FX1 = OFF_FO + 2 * 64 * 32 * 8;
constexpr unsigned OFF_FH = OFF_FX1 + 2 * 64 * 16 * 8;
constexpr unsigned OFF_FX2 = OFF_FH + 2 * 64 * 16 * 8;
constexpr unsigned OFF_FW = OFF_FX2 + 64 * 16 * 8;
constexpr unsigned OFF_CSG = 128 * 1024;
constexpr unsigned OFF_ST1 = 512 * 1024;
constexpr unsigned OFF_ST2 = OFF_ST1 + 2 * 256 * 1024;
constexpr unsigned OFF_WO = OFF_ST2 + 256 * 1024;
constexpr unsigned OFF_WD = OFF_WO + 512 * 1024;
constexpr unsigned OFF_WG = OFF_WD + 1792 * 1024;
constexpr unsigned OFF_Q = 8 * MB;
constexpr unsigned OFF_K = OFF_Q + 4 * MB;
constexpr unsigned OFF_V = OFF_K + 4 * MB;
constexpr unsigned OFF_O = OFF_V + 4 * MB;
constexpr unsigned OFF_X1 = OFF_O + 4 * MB;
constexpr unsigned OFF_X2 = OFF_X1 + 4 * MB;
constexpr unsigned OFF_H = OFF_X2 + 2 * MB;
constexpr unsigned OFF_DBG = OFF_H + 14 * MB;
constexpr unsigned WS_USED = OFF_DBG + 2 * MB;
static_assert(OFF_WG + 3584 * 1024 <= OFF_Q, "workspace map");

constexpr unsigned L_SLOT = 32768;
constexpr unsigned L_Q = 98304;
constexpr unsigned L_CH = L_Q + 8192;
constexpr unsigned L_EX = 131072;
constexpr unsigned L_CSQ = 147456;
constexpr unsigned L_MU = L_CSQ + 1024;
constexpr unsigned L_RS = L_MU + 512;
constexpr unsigned L_CSG = L_RS + 512;
constexpr unsigned L_VOTE = L_CSG + 1024;
constexpr unsigned L_SINK = L_VOTE + 256;
constexpr unsigned L_MX = L_SINK + 256;
constexpr unsigned LDS_BYTES = 163840;
static_assert(L_CH + 2 * 8 * 2304 <= L_CSQ && L_MX + 8192 <= LDS_BYTES, "LDS map");

template <int I> struct IC { static constexpr int v = I; };
template <int B, int E, class Fn> DI void static_for(Fn&& f) {
    if constexpr (E - B == 1) f(IC<B>{});
    else if constexpr (E - B > 1) { static_for<B, (B + E) / 2>(f); static_for<(B + E) / 2, E>(f); }
}
template <class T, class U> DI T bc(U v) { return __builtin_bit_cast(T, v); }
DI unsigned rfl(unsigned v) { return __builtin_amdgcn_readfirstlane(v); }
DI unsigned lane_id() { unsigned l; asm volatile("v_mbcnt_lo_u32_b32 %0, -1, 0\n\tv_mbcnt_hi_u32_b32 %0, -1, %0" : "=v"(l)); return l; }
DI rsrc_t mkrsrc(const void* p, unsigned bytes) { return __builtin_amdgcn_make_buffer_rsrc((void*)p, (short)0, (int)bytes, 0x00020000); }
DI f32x16 mfma32(u32x4 a, u32x4 b, f32x16 c) { return __builtin_amdgcn_mfma_f32_32x32x16_bf16(bc<bf16x8>(a), bc<bf16x8>(b), c, 0, 0, 0); }
DI f32x4 mfma16(u32x4 a, u32x4 b, f32x4 c) { return __builtin_amdgcn_mfma_f32_16x16x32_bf16(bc<bf16x8>(a), bc<bf16x8>(b), c, 0, 0, 0); }
DI f32x16 smfmac(u32x4 a, f32x8 b, f32x16 c, int idx) { return __builtin_amdgcn_smfmac_f32_32x32x32_bf16(bc<bf16x8>(a), bc<bf16x16>(b), c, idx, 0, 0); }
DI unsigned pk2(float lo, float hi) { bf16x2 v = {(__bf16)lo, (__bf16)hi}; return bc<unsigned>(v); }
DI float bflo(unsigned d) { return bc<float>(d << 16); }
DI float bfhi(unsigned d) { return bc<float>(d & 0xffff0000u); }
DI u32x2 swap32(unsigned a, unsigned b) { auto r = __builtin_amdgcn_permlane32_swap(a, b, false, false); return u32x2{r[0], r[1]}; }
DI u32x2 swap16(unsigned a, unsigned b) { auto r = __builtin_amdgcn_permlane16_swap(a, b, false, false); return u32x2{r[0], r[1]}; }
DI float swap32_add(float v) { const u32x2 r = swap32(bc<unsigned>(v), bc<unsigned>(v)); return bc<float>(r.x) + bc<float>(r.y); }
DI float swap32_max(float v) { const u32x2 r = swap32(bc<unsigned>(v), bc<unsigned>(v)); return fmaxf(bc<float>(r.x), bc<float>(r.y)); }
DI u32x4 merge16(u32x2 a, u32x2 b) { const u32x2 x = swap16(a.x, b.x), y = swap16(a.y, b.y); return u32x4{x.x, y.x, x.y, y.y}; }

template <class T> DI T lds_ld(unsigned a) { return *(const LAS T*)(size_t)a; }
template <class T> DI void lds_st(unsigned a, T v) { *(LAS T*)(size_t)a = v; }
DI u32x4 lds_tr(unsigned a, unsigned step) {
    const u32x2 lo = bc<u32x2>(__builtin_amdgcn_ds_read_tr16_b64_v4i16((LAS s16x4*)(size_t)a));
    const u32x2 hi = bc<u32x2>(__builtin_amdgcn_ds_read_tr16_b64_v4i16((LAS s16x4*)(size_t)(a + step)));
    return u32x4{lo.x, lo.y, hi.x, hi.y};
}

template <int AUX> DI u32x4 ld16(rsrc_t r, unsigned vo, unsigned so) { return __builtin_amdgcn_raw_buffer_load_b128(r, vo, so, AUX); }
template <int AUX> DI u32x2 ld8(rsrc_t r, unsigned vo, unsigned so) { return __builtin_amdgcn_raw_buffer_load_b64(r, vo, so, AUX); }
template <int AUX> DI void st16(u32x4 v, rsrc_t r, unsigned vo, unsigned so) { __builtin_amdgcn_raw_buffer_store_b128(v, r, vo, so, AUX); }
template <int AUX> DI void st8(u32x2 v, rsrc_t r, unsigned vo, unsigned so) { __builtin_amdgcn_raw_buffer_store_b64(v, r, vo, so, AUX); }
DI u32x4 ldfrag(rsrc_t r, unsigned vo, unsigned so, int i) { return ld16<SC1>(r, vo + (i & 3) * 1024, so + (i >> 2) * 4096); }
DI void stfrag(u32x4 v, rsrc_t r, unsigned vo, unsigned so, int i) { st16<SC1>(v, r, vo + (i & 3) * 1024, so + (i >> 2) * 4096); }

template <bool DEV, int IMM> DI void dma(rsrc_t r, unsigned vo, unsigned so, unsigned la) {
    if constexpr (DEV) asm volatile("s_mov_b32 m0, %0\n\ts_nop 0\n\tbuffer_load_dwordx4 %1, %2, %3 offen offset:%4 sc1 lds" ::"s"(la), "v"(vo), "s"(r), "s"(so), "i"(IMM) : "m0", "memory");
    else asm volatile("s_mov_b32 m0, %0\n\ts_nop 0\n\tbuffer_load_dwordx4 %1, %2, %3 offen offset:%4 lds" ::"s"(la), "v"(vo), "s"(r), "s"(so), "i"(IMM) : "m0", "memory");
}
DI void warm(rsrc_t r, unsigned vo, unsigned so) {
    asm volatile("s_mov_b32 m0, %0\n\ts_nop 0\n\tbuffer_load_dword %1, %2, %3 offen sc1 lds" ::"s"(L_SINK), "v"(vo), "s"(r), "s"(so) : "m0", "memory");
}
template <int N> DI void vmwait() { asm volatile("s_waitcnt vmcnt(%0)" ::"i"(N) : "memory"); }
DI void bar() { SB(); asm volatile("s_barrier" ::: "memory"); SB(); }
DI void bar_lds() { SB(); asm volatile("s_waitcnt lgkmcnt(0)\n\ts_barrier" ::: "memory"); SB(); }

DI void wait_flags(char* ws, unsigned off, u64 tag) {
    gu64* p = (gu64*)(ws + off);
    for (unsigned sp = 0; sp < (1u << 18); ++sp) {
        const u64 v = __hip_atomic_load(p, RLX_AGENT);
        if (__all(v == tag)) break;
    }
    asm volatile("" ::: "memory");
}
DI void set_flag(char* ws, unsigned off, u64 tag, unsigned lane) {
    vmwait<0>();
    if (lane == 0) __hip_atomic_store((gu64*)(ws + off), tag, RLX_AGENT);
}

#define STAMP(i)
#define STAMP_OUT()

struct Ctx {
    char* ws;
    rsrc_t rws, rx, rwqkv;
    u64 tag;
    unsigned b, w;
    DI unsigned g() const { return b & 7; }
    DI unsigned r() const { return b >> 3; }
    DI unsigned col8() const { return 8 * (b & 1) + ((b >> 3) & 7); }
    DI unsigned quad() const { return 16 * ((b & 7) >> 1) + 4 * (b >> 6); }
    DI unsigned fp() const { return 4 * (b & 1) + ((b >> 3) & 3); }
    DI unsigned pair() const { return 16 * ((b & 7) >> 1) + 2 * (b >> 5); }
};
DI Ctx fresh(const Ctx& c0) { Ctx c = c0; asm volatile("" : "+s"(c.b), "+s"(c.w)); return c; }

DI void finish_stats(float s, float q, float& mu, float& rs) {
    mu = s * (1.f / DM);
    rs = __builtin_amdgcn_rsqf(fmaxf(q * (1.f / DM) - mu * mu, 0.f) + 1e-5f);
}
DI void load_stats(const Ctx& c, unsigned off, unsigned tt, unsigned lane, u32x2 (&sp)[8]) {
    const unsigned vo = (lane & 31) * 8 + (lane >> 5) * 2048;
#pragma unroll
    for (int i = 0; i < 8; ++i) sp[i] = ld8<SC1>(c.rws, vo + i * 256, off + tt * 4096);
}
DI void reduce_stats(const u32x2 (&sp)[8], float& mu, float& rs) {
    float s = 0.f, q = 0.f;
#pragma unroll
    for (int i = 0; i < 8; ++i) { s += bc<float>(sp[i].x); q += bc<float>(sp[i].y); }
    finish_stats(swap32_add(s), swap32_add(q), mu, rs);
}

template <bool FIRST> DI unsigned qkv_w_voff(unsigned lane) {
    const unsigned row = lane >> 2, src = FIRST ? (row >> 2) * 8 + (row & 3) : row;
    return src * 3072 + (((lane & 3) ^ ((row >> 1) & 2)) * 16);
}
template <bool FIRST> DI void qkv_w_piece(const Ctx& c, unsigned vo, unsigned chunk, unsigned p, unsigned la) {
    const unsigned s = p >> 2, pc = p & 3, row0 = 64 * chunk + (FIRST ? 32 * (pc >> 1) + 4 * (pc & 1) : 16 * pc);
    dma<false, 0>(c.rwqkv, vo, row0 * 3072 + s * 1024 + c.col8() * 64, la);
}
DI unsigned qkv_w_lane(unsigned lane) { const unsigned q = lane >> 4; return (4 * q + ((lane & 15) >> 2)) * 64 + (q & 1) * 32 + (lane & 3) * 8; }

struct QkvAcc { f32x4 t[6][2]; };
template <int I> DI void qkv_mma(QkvAcc& a, u32x4 wf, u32x4 x0, u32x4 x1) {
    if constexpr (I < 4) { a.t[I][0] = mfma16(wf, x0, a.t[I][0]); PIN(a.t[I][0]); a.t[I][1] = mfma16(wf, x1, a.t[I][1]); PIN(a.t[I][1]); }
    else { a.t[I][0] = mfma16(x0, wf, a.t[I][0]); PIN(a.t[I][0]); a.t[I][1] = mfma16(x1, wf, a.t[I][1]); PIN(a.t[I][1]); }
}

DI void qkv_epilogue(const Ctx& c, unsigned layer, unsigned tt, unsigned lane, const QkvAcc& a) {
    const unsigned cc = lane & 15, q = lane >> 4, j = c.col8(), tl = c.w * 32;
    u32x2 p[6][2];
#pragma unroll
    for (int h = 0; h < 2; ++h) {
        const float mu = lds_ld<float>(L_MU + (tl + 16 * h + cc) * 4), rs = lds_ld<float>(L_RS + (tl + 16 * h + cc) * 4);
        const float rq = rs * (0.25f * 1.4426950408889634f);
        const f32x4 m4 = lds_ld<f32x4>(L_MU + (tl + 16 * h + 4 * q) * 4), r4 = lds_ld<f32x4>(L_RS + (tl + 16 * h + 4 * q) * 4);
#pragma unroll
        for (int i = 0; i < 4; ++i) {
            const f32x4 cs = lds_ld<f32x4>(L_CSQ + (16 * i + 4 * q) * 4) + lds_ld<f32x4>(L_CSQ + (96 + 16 * i + 4 * q) * 4);
            const f32x4 y = (a.t[i][h] - mu * cs) * (i < 2 ? rq : rs);
            p[i][h] = u32x2{pk2(y[0], y[1]), pk2(y[2], y[3])};
        }
#pragma unroll
        for (int i = 4; i < 6; ++i) {
            const float cs = lds_ld<float>(L_CSQ + (16 * i + cc) * 4) + lds_ld<float>(L_CSQ + (96 + 16 * i + cc) * 4);
            const f32x4 y = (a.t[i][h] - m4 * cs) * r4;
            p[i][h] = u32x2{pk2(y[0], y[1]), pk2(y[2], y[3])};
        }
    }
    const unsigned so = layer * 2 * MB + (2 * j) * 65536 + tt * 1024, t = lane & 31;
    const unsigned vk = (((t & 3) | ((t >> 4) << 2) | (((t >> 2) & 3) << 3)) + (lane & 32)) * 16;
#pragma unroll
    for (int s = 0; s < 2; ++s) {
        st16<SC1>(merge16(p[2 + s][0], p[2 + s][1]), c.rws, vk, OFF_K + so + s * 65536);
        st16<SC1>(u32x4{p[4 + s][0].x, p[4 + s][0].y, p[4 + s][1].x, p[4 + s][1].y}, c.rws, lane * 16, OFF_V + so + s * 65536);
        st16<SC1>(merge16(p[s][0], p[s][1]), c.rws, lane * 16, OFF_Q + so + s * 65536);
    }
    set_flag(c.ws, OFF_FQKV + layer * 8192 + j * 512 + tt * 8, c.tag, lane);
}

DI void phaseA_mfma(const Ctx& c0) {
    const Ctx c = fresh(c0); const unsigned lane = lane_id();
    constexpr int R = 4, P = 3;
    const unsigned w = c.w, cc = lane & 15, q = lane >> 4;
    unsigned wl[4], xa[4];
    wl[0] = 16384 + qkv_w_lane(lane); wl[1] = wl[0] ^ 32; wl[2] = wl[0] + 2 * L_SLOT; wl[3] = wl[1] + 2 * L_SLOT;
    xa[0] = (32 * w + cc) * 128 + ((q ^ (cc >> 1)) * 16); xa[1] = xa[0] ^ 64; xa[2] = xa[0] + 2 * L_SLOT; xa[3] = xa[1] + 2 * L_SLOT;
#pragma unroll
    for (int i = 0; i < 4; ++i) { PIN(wl[i]); PIN(xa[i]); }
    auto wread = [&](int n) AI { const int k = n / 6, i = n % 6, sl = (k >> 1) & 3; return lds_tr(wl[(i & 1) + (sl & 2)] + (sl & 1) * L_SLOT + (i >> 1) * 4096 + (k & 1) * 2048, 1024); };
    auto xread = [&](int k, int h) AI { const int sl = (k >> 1) & 3; return lds_ld<u32x4>(xa[(k & 1) + (sl & 2)] + (sl & 1) * L_SLOT + h * 2048); };
    unsigned one = ONES;
    PIN(one);
    const u32x4 ones = {one, one, one, one};
    QkvAcc a = {};
    f32x4 sx[2] = {}, sq[2] = {};
    u32x4 wf[R], xf[2][2];
    bar();
    xf[0][0] = xread(0, 0); xf[0][1] = xread(0, 1);
#pragma unroll
    for (int n = 0; n < P; ++n) wf[n] = wread(n);
    static_for<0, 96>([&](auto ic) AI {
        constexpr int n = decltype(ic)::v, k = n / 6, i = n % 6;
        if constexpr (n + P < 96) wf[(n + P) % R] = wread(n + P);
        if constexpr (i == 2 && k < 15) { xf[(k + 1) & 1][0] = xread(k + 1, 0); xf[(k + 1) & 1][1] = xread(k + 1, 1); }
        qkv_mma<i>(a, wf[n % R], xf[k & 1][0], xf[k & 1][1]);
        if constexpr (i < 2) { sx[i] = mfma16(ones, xf[k & 1][i], sx[i]); PIN(sx[i]); sq[i] = mfma16(xf[k & 1][i], xf[k & 1][i], sq[i]); PIN(sq[i]); }
        SB();
        if constexpr (n % 12 == 11 && n < 95) bar();
    });
    STAMP(15);
#pragma unroll
    for (int h = 0; h < 2; ++h) {
        const float d = (cc & 2) ? ((cc & 1) ? sq[h][3] : sq[h][2]) : ((cc & 1) ? sq[h][1] : sq[h][0]);
        float mu, rs;
        finish_stats(sx[h][0], d, mu, rs);
        if (q == (cc >> 2)) { lds_st<float>(L_MU + (32 * w + 16 * h + cc) * 4, mu); lds_st<float>(L_RS + (32 * w + 16 * h + cc) * 4, rs); }
    }
    bar_lds();
    qkv_epilogue(c, 0, c.quad() + w, lane, a);
    bar();
}

DI void tile_load(rsrc_t r, unsigned pitch, unsigned rowA, unsigned rowB, bool swap, unsigned col, unsigned la, unsigned lane) {
    const unsigned vo = (lane >> 3) * pitch + (lane & 7) * 16;
#pragma unroll
    for (int p = 0; p < 4; ++p) {
        const unsigned sp = swap ? ((p >> 1) | ((p & 1) << 1)) : p;
        dma<false, 0>(r, vo, (((sp & 2) ? rowB : rowA) + 8 * (sp & 1)) * pitch + col * 2, la + p * 1024);
    }
}
DI u32x4 tile_frag(unsigned la, int ct, unsigned lane) { return lds_tr(la + (4 * (lane >> 4) + ((lane & 15) >> 2)) * 128 + (lane & 3) * 8 + ct * 32, 2048); }

DI void phaseA_help(const Ctx& c0, const void* wo, const void* wg, const void* wu, const void* wd) {
    const Ctx c = fresh(c0); const unsigned lane = lane_id();
    const unsigned hw = c.w - 4, tok0 = 32 * (c.quad() + hw);
    const unsigned vx0 = (lane >> 3) * 1024 + (((lane & 7) ^ (lane >> 4)) * 16), vx1 = vx0 ^ 64, vw = qkv_w_voff<true>(lane);
    auto chunk = [&](int n) AI {
        const unsigned sl = (n & 3) * L_SLOT;
#pragma unroll
        for (int p = 0; p < 4; ++p) dma<false, 0>(c.rx, (p & 1) ? vx1 : vx0, (tok0 + 8 * p) * 1024 + n * 128, sl + hw * 4096 + p * 1024);
#pragma unroll
        for (int p = 0; p < 3; ++p) qkv_w_piece<true>(c, vw, n, 3 * hw + p, sl + 16384 + (3 * hw + p) * 1024);
    };
    chunk(0); chunk(1); chunk(2); chunk(3);
    unsigned one = ONES;
    PIN(one);
    const u32x4 ones = {one, one, one, one};
    unsigned wl[3];
#pragma unroll
    for (int i = 0; i < 3; ++i) { const unsigned ti = 3 * (hw & 1) + i; wl[i] = 16384 + (qkv_w_lane(lane) ^ ((ti & 1) * 32)) + (ti >> 1) * 4096 + (hw >> 1) * 2048; }
    f32x4 cs[3] = {};
    const unsigned gh = c.b * 4 + hw, stA = hw * L_SLOT + 28672, stB = L_EX + hw * 4096;
    static_for<0, 8>([&](auto ic) AI {
        constexpr int n = decltype(ic)::v;
        if constexpr (n == 0) vmwait<14>(); else if constexpr (n <= 5) vmwait<7>();
        else if constexpr (n == 6) { if (gh < 448) vmwait<8>(); else vmwait<4>(); }
        bar_lds();
        if constexpr (n >= 1 && n <= 4) chunk(n + 3);
        if constexpr (n == 5) {
            if (gh < 896) { const unsigned m = gh / 448, u = gh % 448; tile_load(mkrsrc(m ? wu : wg, DM * FF * 2), FF * 2, 32 * (u & 15), 32 * (u & 15) + 16, false, 64 * (u >> 4), stA, lane); }
            else { const unsigned u = gh - 896; tile_load(mkrsrc(wo, DM * DM * 2), DM * 2, 32 * (u & 15), 32 * (u & 15) + 16, true, 64 * (u >> 4), stA, lane); }
            if (gh < 448) {
                const unsigned sg = gh % 56, pr = sg / 7, e = sg % 7, fa = 14 * pr + (e < 3 ? 2 * e : e < 6 ? 2 * e + 1 : 6), fb = e < 6 ? fa + 1 : fa + 7;
                tile_load(mkrsrc(wd, FF * DM * 2), DM * 2, 16 * fa, 16 * fb, false, 64 * (gh / 56), stB, lane);
            }
        }
#pragma unroll
        for (int i = 0; i < 3; ++i) { cs[i] = mfma16(ones, lds_tr(wl[i] + (n & 3) * L_SLOT, 1024), cs[i]); PIN(cs[i]); }
    });
    if (lane < 16) {
#pragma unroll
        for (int i = 0; i < 3; ++i) lds_st<float>(L_CSQ + ((hw >> 1) * 96 + 48 * (hw & 1) + 16 * i + lane) * 4, cs[i][0]);
    }
    bar_lds();
    vmwait<0>();
    const unsigned vo = lane * 16;
    if (gh < 896) {
        const unsigned m = gh / 448, u = gh % 448, kb = u & 15, cb = u >> 4;
        float s[4];
#pragma unroll
        for (int ct = 0; ct < 4; ++ct) {
            const u32x4 f = tile_frag(stA, ct, lane);
            const unsigned fb = 4 * cb + ct;
            st16<SC1>(f, c.rws, vo, OFF_WG + ((((fb / 7) * 16 + kb) * 7 + fb % 7) * 2 + m) * 1024);
            s[ct] = mfma16(ones, f, f32x4{})[0];
        }
        const float sv = (lane & 32) ? ((lane & 16) ? s[3] : s[2]) : ((lane & 16) ? s[1] : s[0]);
        __builtin_amdgcn_raw_buffer_store_b32(bc<unsigned>(sv), c.rws, lane * 4, OFF_CSG + (kb * 2 + m) * 7168 + cb * 256, SC1);
    } else {
        const unsigned u = gh - 896, kb = u & 15, cb = u >> 4;
#pragma unroll
        for (int ct = 0; ct < 4; ++ct) stfrag(tile_frag(stA, ct, lane), c.rws, vo, OFF_WO + ((cb * 8 + (kb & 7)) * 2 + (kb >> 3)) * 4096, ct);
    }
    if (gh < 448) {
        const unsigned sg = gh % 56;
#pragma unroll
        for (int ct = 0; ct < 4; ++ct) stfrag(tile_frag(stB, ct, lane), c.rws, vo, OFF_WD + (((gh / 56) * 28 + sg % 28) * 2 + sg / 28) * 4096, ct);
    }
    vmwait<0>();
    bar_lds();
    if (hw == 3 && lane == 0) __hip_atomic_store((gu64*)(c.ws + OFF_FW + c.b * 8), lds_ld<u64>(L_VOTE + 8), RLX_AGENT);
}

DI void phaseF(const Ctx& c0) {
    const Ctx c = fresh(c0); const unsigned lane = lane_id();
    constexpr int R = 5, P = 4;
    const unsigned w = c.w, tt = c.quad() + w, vo = lane * 16;
    wait_flags(c.ws, OFF_FX2 + tt * 128 + (lane & 15) * 8, c.tag);
    STAMP(27);
    u32x4 tok[32], wf[R];
    u32x2 sp[8];
#pragma unroll
    for (int s = 0; s < 8; ++s) tok[s] = ldfrag(c.rws, vo, OFF_X2 + tt * 32768, s);
    load_stats(c, OFF_ST2, tt, lane, sp);
#pragma unroll
    for (int s = 8; s < 32; ++s) tok[s] = ldfrag(c.rws, vo, OFF_X2 + tt * 32768, s);
    bar();
    unsigned wl[4];
    wl[0] = L_SLOT + qkv_w_lane(lane); wl[1] = wl[0] ^ 32; wl[2] = wl[0] + 49152; wl[3] = wl[1] + 49152;
#pragma unroll
    for (int i = 0; i < 4; ++i) PIN(wl[i]);
    auto wread = [&](int n) AI { const int k = n / 6, i = n % 6; return lds_tr(wl[(i & 1) + ((k >> 3) << 1)] + ((k >> 1) & 3) * 12288 + (i >> 1) * 4096 + (k & 1) * 2048, 1024); };
#pragma unroll
    for (int n = 0; n < P; ++n) wf[n] = wread(n);
    QkvAcc a = {};
    static_for<0, 96>([&](auto ic) AI {
        constexpr int n = decltype(ic)::v, k = n / 6, i = n % 6;
        if constexpr (n + P < 96) wf[(n + P) % R] = wread(n + P);
        qkv_mma<i>(a, wf[n % R], tok[2 * k], tok[2 * k + 1]);
        if constexpr (n == 30) {
            float mu, rs;
            reduce_stats(sp, mu, rs);
            if (lane < 32) { lds_st<float>(L_MU + (32 * w + lane) * 4, mu); lds_st<float>(L_RS + (32 * w + lane) * 4, rs); }
        }
        SB();
    });
    STAMP(28);
    bar_lds();
    qkv_epilogue(c, 1, tt, lane, a);
}

template <int N> DI void issue_chunk(const Ctx& c, unsigned lane) {
    constexpr int np = (N >= 2 && N <= 9) ? 7 : 8;
    const unsigned hw = c.w - 4;
    const unsigned src = (N < 2 ? OFF_WO + c.fp() * 65536 + N * 32768 : N < 10 ? OFF_WG + c.col8() * 229376 + (N - 2) * 28672 : OFF_WD + c.fp() * 229376 + (N - 10) * 32768) + hw * (np * 1024);
    const unsigned dst = (N & 3) * L_SLOT + hw * (np * 1024), vo = lane * 16;
    dma<true, 0>(c.rws, vo, src, dst); dma<true, 1024>(c.rws, vo, src, dst); dma<true, 2048>(c.rws, vo, src, dst); dma<true, 3072>(c.rws, vo, src, dst);
    dma<true, 0>(c.rws, vo, src + 4096, dst + 4096); dma<true, 1024>(c.rws, vo, src + 4096, dst + 4096); dma<true, 2048>(c.rws, vo, src + 4096, dst + 4096);
    if constexpr (np == 8) dma<true, 3072>(c.rws, vo, src + 4096, dst + 4096);
}

#define ATT_FILL \
    "v_mov_b32 v160, %[cv]\n\tv_mov_b32 v161, %[cv]\n\tv_mov_b32 v162, %[cv]\n\tv_mov_b32 v163, %[cv]\n\tv_mov_b32 v164, %[cv]\n\tv_mov_b32 v165, %[cv]\n\tv_mov_b32 v166, %[cv]\n\t" \
    "v_mov_b32 v167, %[cv]\n\tv_mov_b32 v168, %[cv]\n\tv_mov_b32 v169, %[cv]\n\tv_mov_b32 v170, %[cv]\n\tv_mov_b32 v171, %[cv]\n\tv_mov_b32 v172, %[cv]\n\tv_mov_b32 v173, %[cv]\n\t" \
    "v_mov_b32 v174, %[cv]\n\tv_mov_b32 v175, %[cv]\n\tv_mov_b32 v176, %[cv]\n\tv_mov_b32 v177, %[cv]\n\tv_mov_b32 v178, %[cv]\n\tv_mov_b32 v179, %[cv]\n\tv_mov_b32 v180, %[cv]\n\t" \
    "v_mov_b32 v181, %[cv]\n\tv_mov_b32 v182, %[cv]\n\tv_mov_b32 v183, %[cv]\n\tv_mov_b32 v184, %[cv]\n\tv_mov_b32 v185, %[cv]\n\tv_mov_b32 v186, %[cv]\n\tv_mov_b32 v187, %[cv]\n\t" \
    "v_mov_b32 v188, %[cv]\n\tv_mov_b32 v189, %[cv]\n\tv_mov_b32 v190, %[cv]\n\tv_mov_b32 v191, %[cv]\n\tv_mov_b32 v192, %[cv]\n\tv_mov_b32 v193, %[cv]\n\tv_mov_b32 v194, %[cv]\n\t" \
    "v_mov_b32 v195, %[cv]\n\tv_mov_b32 v196, %[cv]\n\tv_mov_b32 v197, %[cv]\n\tv_mov_b32 v198, %[cv]\n\tv_mov_b32 v199, %[cv]\n\tv_mov_b32 v200, %[cv]\n\tv_mov_b32 v201, %[cv]\n\t" \
    "v_mov_b32 v202, %[cv]\n\tv_mov_b32 v203, %[cv]\n\tv_mov_b32 v204, %[cv]\n\tv_mov_b32 v205, %[cv]\n\tv_mov_b32 v206, %[cv]\n\tv_mov_b32 v207, %[cv]\n\tv_mov_b32 v208, %[cv]\n\t" \
    "v_mov_b32 v209, %[cv]\n\tv_mov_b32 v210, %[cv]\n\tv_mov_b32 v211, %[cv]\n\tv_mov_b32 v212, %[cv]\n\tv_mov_b32 v213, %[cv]\n\tv_mov_b32 v214, %[cv]\n\tv_mov_b32 v215, %[cv]\n\t" \
    "v_mov_b32 v216, %[cv]\n\tv_mov_b32 v217, %[cv]\n\tv_mov_b32 v218, %[cv]\n\tv_mov_b32 v219, %[cv]\n\tv_mov_b32 v220, %[cv]\n\tv_mov_b32 v221, %[cv]\n\tv_mov_b32 v222, %[cv]\n\t" \
    "v_mov_b32 v223, %[cv]\n\tv_mov_b32 v48, 0\n\tv_mov_b32 v49, 0\n\tv_mov_b32 v50, 0\n\tv_mov_b32 v51, 0\n\tv_mov_b32 v52, 0\n\tv_mov_b32 v53, 0\n\tv_mov_b32 v54, 0\n\tv_mov_b32 v55, 0\n\t" \
    "v_mov_b32 v56, 0\n\tv_mov_b32 v57, 0\n\tv_mov_b32 v58, 0\n\tv_mov_b32 v59, 0\n\tv_mov_b32 v60, 0\n\tv_mov_b32 v61, 0\n\tv_mov_b32 v62, 0\n\tv_mov_b32 v63, 0\n\tv_mov_b32 v64, 0\n\t" \
    "v_mov_b32 v65, 0\n\tv_mov_b32 v66, 0\n\tv_mov_b32 v67, 0\n\tv_mov_b32 v68, 0\n\tv_mov_b32 v69, 0\n\tv_mov_b32 v70, 0\n\tv_mov_b32 v71, 0\n\tv_mov_b32 v72, 0\n\tv_mov_b32 v73, 0\n\t" \
    "v_mov_b32 v74, 0\n\tv_mov_b32 v75, 0\n\tv_mov_b32 v76, 0\n\tv_mov_b32 v77, 0\n\tv_mov_b32 v78, 0\n\tv_mov_b32 v79, 0\n\t"
#define ATT_FIRST \
    "s_nop 4\n\ts_mov_b32 m0, %[qd]\n\ts_nop 0\n\tbuffer_load_dwordx4 v95, %[rs], %[sq] offen sc1 lds\n\tbuffer_load_dwordx4 v[80:83], v95, %[rs], %[sq] offen sc1\n\t" \
    "buffer_load_dwordx4 v[128:131], v95, %[rs], %[sk] offen offset:0 sc1\n\ts_mov_b64 exec, %[vm]\n\tbuffer_load_dwordx4 v[160:163], v96, %[rs], %[sv] offen offset:0 sc1\n\t" \
    "buffer_load_dwordx4 v[164:167], v96, %[rs], %[sv] offen offset:512 sc1\n\ts_mov_b64 exec, -1\n\tbuffer_load_dwordx4 v[132:135], v95, %[rs], %[sk] offen offset:1024 sc1\n\t" \
    "s_mov_b64 exec, %[vm]\n\tbuffer_load_dwordx4 v[168:171], v96, %[rs], %[sv] offen offset:1024 sc1\n\tbuffer_load_dwordx4 v[172:175], v96, %[rs], %[sv] offen offset:1536 sc1\n\t" \
    "s_mov_b64 exec, -1\n\tbuffer_load_dwordx4 v[136:139], v95, %[rs], %[sk] offen offset:2048 sc1\n\ts_mov_b64 exec, %[vm]\n\t" \
    "buffer_load_dwordx4 v[176:179], v96, %[rs], %[sv] offen offset:2048 sc1\n\tbuffer_load_dwordx4 v[180:183], v96, %[rs], %[sv] offen offset:2560 sc1\n\ts_mov_b64 exec, -1\n\t" \
    "buffer_load_dwordx4 v[140:143], v95, %[rs], %[sk] offen offset:3072 sc1\n\ts_mov_b64 exec, %[vm]\n\tbuffer_load_dwordx4 v[184:187], v96, %[rs], %[sv] offen offset:3072 sc1\n\t" \
    "buffer_load_dwordx4 v[188:191], v96, %[rs], %[sv] offen offset:3584 sc1\n\ts_mov_b64 exec, -1\n\ts_add_u32 %[sk], %[sk], 0x1000\n\ts_add_u32 %[sv], %[sv], 0x1000\n\t" \
    "buffer_load_dwordx4 v[144:147], v95, %[rs], %[sk] offen offset:0 sc1\n\ts_mov_b64 exec, %[vm]\n\tbuffer_load_dwordx4 v[192:195], v96, %[rs], %[sv] offen offset:0 sc1\n\t" \
    "buffer_load_dwordx4 v[196:199], v96, %[rs], %[sv] offen offset:512 sc1\n\ts_mov_b64 exec, -1\n\tbuffer_load_dwordx4 v[148:151], v95, %[rs], %[sk] offen offset:1024 sc1\n\t" \
    "s_mov_b64 exec, %[vm]\n\tbuffer_load_dwordx4 v[200:203], v96, %[rs], %[sv] offen offset:1024 sc1\n\tbuffer_load_dwordx4 v[204:207], v96, %[rs], %[sv] offen offset:1536 sc1\n\t" \
    "s_mov_b64 exec, -1\n\tbuffer_load_dwordx4 v[152:155], v95, %[rs], %[sk] offen offset:2048 sc1\n\ts_mov_b64 exec, %[vm]\n\t" \
    "buffer_load_dwordx4 v[208:211], v96, %[rs], %[sv] offen offset:2048 sc1\n\tbuffer_load_dwordx4 v[212:215], v96, %[rs], %[sv] offen offset:2560 sc1\n\ts_mov_b64 exec, -1\n\t" \
    "buffer_load_dwordx4 v[156:159], v95, %[rs], %[sk] offen offset:3072 sc1\n\ts_mov_b64 exec, %[vm]\n\tbuffer_load_dwordx4 v[216:219], v96, %[rs], %[sv] offen offset:3072 sc1\n\t" \
    "buffer_load_dwordx4 v[220:223], v96, %[rs], %[sv] offen offset:3584 sc1\n\ts_mov_b64 exec, -1\n\ts_waitcnt vmcnt(23)\n\tv_mfma_f32_32x32x16_bf16 v[16:31], v[128:131], v[80:83], 0\n\t" \
    "s_nop 15\n\tv_exp_f32 v16, v16\n\tv_exp_f32 v17, v17\n\tv_exp_f32 v18, v18\n\tv_exp_f32 v19, v19\n\tv_exp_f32 v20, v20\n\ts_waitcnt vmcnt(20)\n\t" \
    "v_mfma_f32_32x32x16_bf16 v[32:47], v[132:135], v[80:83], 0\n\tv_exp_f32 v21, v21\n\tv_exp_f32 v22, v22\n\tv_exp_f32 v23, v23\n\tv_exp_f32 v24, v24\n\tv_exp_f32 v25, v25\n\t" \
    "v_exp_f32 v26, v26\n\tv_smfmac_f32_32x32x32_bf16 v[48:63], v[160:163], v[16:23], v94\n\tv_exp_f32 v27, v27\n\tv_exp_f32 v28, v28\n\tv_exp_f32 v29, v29\n\tv_exp_f32 v30, v30\n\t" \
    "v_exp_f32 v31, v31\n\tv_exp_f32 v32, v32\n\tv_exp_f32 v33, v33\n\tv_smfmac_f32_32x32x32_bf16 v[48:63], v[164:167], v[24:31], v94\n\tds_write_b128 v88, v[64:67] offset:18432\n\t" \
    "ds_write_b128 v88, v[68:71] offset:19456\n\tds_write_b32 v89, v72 offset:20480\n\tv_exp_f32 v34, v34\n\tv_exp_f32 v35, v35\n\tv_exp_f32 v36, v36\n\ts_waitcnt vmcnt(17)\n\t" \
    "v_mfma_f32_32x32x16_bf16 v[16:31], v[136:139], v[80:83], 0\n\tv_exp_f32 v37, v37\n\tv_exp_f32 v38, v38\n\tv_exp_f32 v39, v39\n\tv_exp_f32 v40, v40\n\tv_exp_f32 v41, v41\n\t" \
    "v_exp_f32 v42, v42\n\tv_smfmac_f32_32x32x32_bf16 v[48:63], v[168:171], v[32:39], v94\n\tv_exp_f32 v43, v43\n\tv_exp_f32 v44, v44\n\tv_exp_f32 v45, v45\n\tv_exp_f32 v46, v46\n\t" \
    "v_exp_f32 v47, v47\n\tv_exp_f32 v16, v16\n\tv_exp_f32 v17, v17\n\tv_smfmac_f32_32x32x32_bf16 v[48:63], v[172:175], v[40:47], v94\n\tv_exp_f32 v18, v18\n\tv_exp_f32 v19, v19\n\t" \
    "v_exp_f32 v20, v20\n\ts_waitcnt vmcnt(14)\n\tv_mfma_f32_32x32x16_bf16 v[32:47], v[140:143], v[80:83], 0\n\tv_exp_f32 v21, v21\n\tv_exp_f32 v22, v22\n\tv_exp_f32 v23, v23\n\t" \
    "v_exp_f32 v24, v24\n\tv_exp_f32 v25, v25\n\tv_exp_f32 v26, v26\n\tv_smfmac_f32_32x32x32_bf16 v[48:63], v[176:179], v[16:23], v94\n\tv_exp_f32 v27, v27\n\tv_exp_f32 v28, v28\n\t" \
    "v_exp_f32 v29, v29\n\tv_exp_f32 v30, v30\n\tv_exp_f32 v31, v31\n\tv_exp_f32 v32, v32\n\tv_exp_f32 v33, v33\n\tv_smfmac_f32_32x32x32_bf16 v[48:63], v[180:183], v[24:31], v94\n\t" \
    "v_exp_f32 v34, v34\n\tv_exp_f32 v35, v35\n\tv_exp_f32 v36, v36\n\ts_waitcnt vmcnt(11)\n\tv_mfma_f32_32x32x16_bf16 v[16:31], v[144:147], v[80:83], 0\n\tv_exp_f32 v37, v37\n\t" \
    "v_exp_f32 v38, v38\n\tv_exp_f32 v39, v39\n\tv_exp_f32 v40, v40\n\tv_exp_f32 v41, v41\n\tv_exp_f32 v42, v42\n\tv_smfmac_f32_32x32x32_bf16 v[48:63], v[184:187], v[32:39], v94\n\t" \
    "v_exp_f32 v43, v43\n\tv_exp_f32 v44, v44\n\tv_exp_f32 v45, v45\n\tv_exp_f32 v46, v46\n\tv_exp_f32 v47, v47\n\tv_exp_f32 v16, v16\n\tv_exp_f32 v17, v17\n\t" \
    "v_smfmac_f32_32x32x32_bf16 v[48:63], v[188:191], v[40:47], v94\n\ts_waitcnt lgkmcnt(0)\n\ts_barrier\n\tds_read_b128 v[64:67], v90 offset:18432\n\tds_read_b128 v[68:71], v90 offset:19456\n\t" \
    "ds_read_b32 v72, v91 offset:20480\n\ts_add_u32 %[qo], %[qo], 0x400\n\ts_and_b32 %[qo], %[qo], 0x1fff\n\tv_add_u32 v97, %[qo], v98\n\tds_read_b128 v[84:87], v97\n\tv_exp_f32 v18, v18\n\t" \
    "v_exp_f32 v19, v19\n\tv_exp_f32 v20, v20\n\ts_waitcnt vmcnt(8)\n\tv_mfma_f32_32x32x16_bf16 v[32:47], v[148:151], v[80:83], 0\n\tv_exp_f32 v21, v21\n\tv_exp_f32 v22, v22\n\t" \
    "v_exp_f32 v23, v23\n\tv_exp_f32 v24, v24\n\tv_exp_f32 v25, v25\n\tv_exp_f32 v26, v26\n\tv_smfmac_f32_32x32x32_bf16 v[48:63], v[192:195], v[16:23], v94\n\tv_exp_f32 v27, v27\n\t" \
    "v_exp_f32 v28, v28\n\tv_exp_f32 v29, v29\n\tv_exp_f32 v30, v30\n\tv_exp_f32 v31, v31\n\tv_exp_f32 v32, v32\n\tv_exp_f32 v33, v33\n\t" \
    "v_smfmac_f32_32x32x32_bf16 v[48:63], v[196:199], v[24:31], v94\n\tv_exp_f32 v34, v34\n\tv_exp_f32 v35, v35\n\tv_exp_f32 v36, v36\n\ts_waitcnt vmcnt(5)\n\t" \
    "v_mfma_f32_32x32x16_bf16 v[16:31], v[152:155], v[80:83], 0\n\tv_exp_f32 v37, v37\n\tv_exp_f32 v38, v38\n\tv_exp_f32 v39, v39\n\tv_exp_f32 v40, v40\n\tv_exp_f32 v41, v41\n\t" \
    "v_exp_f32 v42, v42\n\tv_smfmac_f32_32x32x32_bf16 v[48:63], v[200:203], v[32:39], v94\n\tv_exp_f32 v43, v43\n\tv_exp_f32 v44, v44\n\tv_exp_f32 v45, v45\n\tv_exp_f32 v46, v46\n\t" \
    "v_exp_f32 v47, v47\n\tv_exp_f32 v16, v16\n\tv_exp_f32 v17, v17\n\tv_smfmac_f32_32x32x32_bf16 v[48:63], v[204:207], v[40:47], v94\n\tv_exp_f32 v18, v18\n\tv_exp_f32 v19, v19\n\t" \
    "v_exp_f32 v20, v20\n\ts_waitcnt vmcnt(2)\n\tv_mfma_f32_32x32x16_bf16 v[32:47], v[156:159], v[80:83], 0\n\tv_exp_f32 v21, v21\n\tv_exp_f32 v22, v22\n\tv_exp_f32 v23, v23\n\t" \
    "v_exp_f32 v24, v24\n\tv_exp_f32 v25, v25\n\tv_exp_f32 v26, v26\n\tv_smfmac_f32_32x32x32_bf16 v[48:63], v[208:211], v[16:23], v94\n\tv_exp_f32 v27, v27\n\tv_exp_f32 v28, v28\n\t" \
    "v_exp_f32 v29, v29\n\tv_exp_f32 v30, v30\n\tv_exp_f32 v31, v31\n\tv_exp_f32 v32, v32\n\tv_exp_f32 v33, v33\n\tv_smfmac_f32_32x32x32_bf16 v[48:63], v[212:215], v[24:31], v94\n\t" \
    "v_exp_f32 v34, v34\n\tv_exp_f32 v35, v35\n\tv_exp_f32 v36, v36\n\ts_waitcnt vmcnt(0)\n\ts_waitcnt lgkmcnt(0)\n\tv_mfma_f32_32x32x16_bf16 v[16:31], v[128:131], v[84:87], 0\n\t" \
    "v_exp_f32 v37, v37\n\tv_exp_f32 v38, v38\n\tv_exp_f32 v39, v39\n\tv_exp_f32 v40, v40\n\tv_exp_f32 v41, v41\n\tv_exp_f32 v42, v42\n\t" \
    "v_smfmac_f32_32x32x32_bf16 v[48:63], v[216:219], v[32:39], v94\n\tv_exp_f32 v43, v43\n\tv_exp_f32 v44, v44\n\tv_exp_f32 v45, v45\n\tv_exp_f32 v46, v46\n\tv_exp_f32 v47, v47\n\t" \
    "v_exp_f32 v16, v16\n\tv_exp_f32 v17, v17\n\tv_smfmac_f32_32x32x32_bf16 v[48:63], v[220:223], v[40:47], v94\n\tv_exp_f32 v18, v18\n\tv_exp_f32 v19, v19\n\tv_exp_f32 v20, v20\n\t" \
    "v_mfma_f32_32x32x16_bf16 v[32:47], v[132:135], v[84:87], 0\n\tv_exp_f32 v21, v21\n\tv_exp_f32 v22, v22\n\tv_exp_f32 v23, v23\n\tv_exp_f32 v24, v24\n\tv_exp_f32 v25, v25\n\t" \
    "v_exp_f32 v26, v26\n\tv_smfmac_f32_32x32x32_bf16 v[64:79], v[160:163], v[16:23], v94\n\tv_exp_f32 v27, v27\n\tv_exp_f32 v28, v28\n\tv_exp_f32 v29, v29\n\tv_exp_f32 v30, v30\n\t" \
    "v_exp_f32 v31, v31\n\tv_exp_f32 v32, v32\n\tv_exp_f32 v33, v33\n\tv_smfmac_f32_32x32x32_bf16 v[64:79], v[164:167], v[24:31], v94\n\tds_write_b128 v88, v[48:51] offset:0\n\t" \
    "ds_write_b128 v88, v[52:55] offset:1024\n\tds_write_b32 v89, v56 offset:2048\n\tv_exp_f32 v34, v34\n\tv_exp_f32 v35, v35\n\tv_exp_f32 v36, v36\n\t" \
    "v_mfma_f32_32x32x16_bf16 v[16:31], v[136:139], v[84:87], 0\n\tv_exp_f32 v37, v37\n\tv_exp_f32 v38, v38\n\tv_exp_f32 v39, v39\n\tv_exp_f32 v40, v40\n\tv_exp_f32 v41, v41\n\t" \
    "v_exp_f32 v42, v42\n\tv_smfmac_f32_32x32x32_bf16 v[64:79], v[168:171], v[32:39], v94\n\tv_exp_f32 v43, v43\n\tv_exp_f32 v44, v44\n\tv_exp_f32 v45, v45\n\tv_exp_f32 v46, v46\n\t" \
    "v_exp_f32 v47, v47\n\tv_exp_f32 v16, v16\n\tv_exp_f32 v17, v17\n\tv_smfmac_f32_32x32x32_bf16 v[64:79], v[172:175], v[40:47], v94\n\tv_exp_f32 v18, v18\n\tv_exp_f32 v19, v19\n\t" \
    "v_exp_f32 v20, v20\n\tv_mfma_f32_32x32x16_bf16 v[32:47], v[140:143], v[84:87], 0\n\tv_exp_f32 v21, v21\n\tv_exp_f32 v22, v22\n\tv_exp_f32 v23, v23\n\tv_exp_f32 v24, v24\n\t" \
    "v_exp_f32 v25, v25\n\tv_exp_f32 v26, v26\n\tv_smfmac_f32_32x32x32_bf16 v[64:79], v[176:179], v[16:23], v94\n\tv_exp_f32 v27, v27\n\tv_exp_f32 v28, v28\n\tv_exp_f32 v29, v29\n\t" \
    "v_exp_f32 v30, v30\n\tv_exp_f32 v31, v31\n\tv_exp_f32 v32, v32\n\tv_exp_f32 v33, v33\n\tv_smfmac_f32_32x32x32_bf16 v[64:79], v[180:183], v[24:31], v94\n\tv_exp_f32 v34, v34\n\t" \
    "v_exp_f32 v35, v35\n\tv_exp_f32 v36, v36\n\tv_mfma_f32_32x32x16_bf16 v[16:31], v[144:147], v[84:87], 0\n\tv_exp_f32 v37, v37\n\tv_exp_f32 v38, v38\n\tv_exp_f32 v39, v39\n\t" \
    "v_exp_f32 v40, v40\n\tv_exp_f32 v41, v41\n\tv_exp_f32 v42, v42\n\tv_smfmac_f32_32x32x32_bf16 v[64:79], v[184:187], v[32:39], v94\n\tv_exp_f32 v43, v43\n\tv_exp_f32 v44, v44\n\t" \
    "v_exp_f32 v45, v45\n\tv_exp_f32 v46, v46\n\tv_exp_f32 v47, v47\n\tv_exp_f32 v16, v16\n\tv_exp_f32 v17, v17\n\tv_smfmac_f32_32x32x32_bf16 v[64:79], v[188:191], v[40:47], v94\n\t" \
    "s_waitcnt lgkmcnt(0)\n\ts_barrier\n\tds_read_b128 v[48:51], v92 offset:0\n\tds_read_b128 v[52:55], v92 offset:1024\n\tds_read_b32 v56, v93 offset:2048\n\ts_add_u32 %[qo], %[qo], 0x400\n\t" \
    "s_and_b32 %[qo], %[qo], 0x1fff\n\tv_add_u32 v97, %[qo], v98\n\tds_read_b128 v[80:83], v97\n\tbuffer_load_dwordx2 v[100:101], v108, %[rs], %[sfw] offen offset:0 sc1\n\t" \
    "buffer_load_dwordx2 v[102:103], v108, %[rs], %[sfw] offen offset:512 sc1\n\tbuffer_load_dwordx2 v[104:105], v108, %[rs], %[sfw] offen offset:1024 sc1\n\t" \
    "buffer_load_dwordx2 v[106:107], v108, %[rs], %[sfw] offen offset:1536 sc1\n\tv_exp_f32 v18, v18\n\tv_exp_f32 v19, v19\n\tv_exp_f32 v20, v20\n\t" \
    "v_mfma_f32_32x32x16_bf16 v[32:47], v[148:151], v[84:87], 0\n\tv_exp_f32 v21, v21\n\tv_exp_f32 v22, v22\n\tv_exp_f32 v23, v23\n\tv_exp_f32 v24, v24\n\tv_exp_f32 v25, v25\n\t" \
    "v_exp_f32 v26, v26\n\tv_smfmac_f32_32x32x32_bf16 v[64:79], v[192:195], v[16:23], v94\n\tv_exp_f32 v27, v27\n\tv_exp_f32 v28, v28\n\tv_exp_f32 v29, v29\n\tv_exp_f32 v30, v30\n\t" \
    "v_exp_f32 v31, v31\n\tv_exp_f32 v32, v32\n\tv_exp_f32 v33, v33\n\tv_smfmac_f32_32x32x32_bf16 v[64:79], v[196:199], v[24:31], v94\n\tv_exp_f32 v34, v34\n\tv_exp_f32 v35, v35\n\t" \
    "v_exp_f32 v36, v36\n\tv_mfma_f32_32x32x16_bf16 v[16:31], v[152:155], v[84:87], 0\n\tv_exp_f32 v37, v37\n\tv_exp_f32 v38, v38\n\tv_exp_f32 v39, v39\n\tv_exp_f32 v40, v40\n\t" \
    "v_exp_f32 v41, v41\n\tv_exp_f32 v42, v42\n\tv_smfmac_f32_32x32x32_bf16 v[64:79], v[200:203], v[32:39], v94\n\tv_exp_f32 v43, v43\n\tv_exp_f32 v44, v44\n\tv_exp_f32 v45, v45\n\t" \
    "v_exp_f32 v46, v46\n\tv_exp_f32 v47, v47\n\tv_exp_f32 v16, v16\n\tv_exp_f32 v17, v17\n\tv_smfmac_f32_32x32x32_bf16 v[64:79], v[204:207], v[40:47], v94\n\tv_exp_f32 v18, v18\n\t" \
    "v_exp_f32 v19, v19\n\tv_exp_f32 v20, v20\n\tv_mfma_f32_32x32x16_bf16 v[32:47], v[156:159], v[84:87], 0\n\tv_exp_f32 v21, v21\n\tv_exp_f32 v22, v22\n\tv_exp_f32 v23, v23\n\t" \
    "v_exp_f32 v24, v24\n\tv_exp_f32 v25, v25\n\tv_exp_f32 v26, v26\n\tv_smfmac_f32_32x32x32_bf16 v[64:79], v[208:211], v[16:23], v94\n\tv_exp_f32 v27, v27\n\tv_exp_f32 v28, v28\n\t" \
    "v_exp_f32 v29, v29\n\tv_exp_f32 v30, v30\n\tv_exp_f32 v31, v31\n\tv_exp_f32 v32, v32\n\tv_exp_f32 v33, v33\n\tv_smfmac_f32_32x32x32_bf16 v[64:79], v[212:215], v[24:31], v94\n\t" \
    "v_exp_f32 v34, v34\n\tv_exp_f32 v35, v35\n\tv_exp_f32 v36, v36\n\ts_waitcnt lgkmcnt(0)\n\tv_mfma_f32_32x32x16_bf16 v[16:31], v[128:131], v[80:83], 0\n\tv_exp_f32 v37, v37\n\t" \
    "v_exp_f32 v38, v38\n\tv_exp_f32 v39, v39\n\tv_exp_f32 v40, v40\n\tv_exp_f32 v41, v41\n\tv_exp_f32 v42, v42\n\tv_smfmac_f32_32x32x32_bf16 v[64:79], v[216:219], v[32:39], v94\n\t" \
    "v_exp_f32 v43, v43\n\tv_exp_f32 v44, v44\n\tv_exp_f32 v45, v45\n\tv_exp_f32 v46, v46\n\tv_exp_f32 v47, v47\n\tv_exp_f32 v16, v16\n\tv_exp_f32 v17, v17\n\t" \
    "v_smfmac_f32_32x32x32_bf16 v[64:79], v[220:223], v[40:47], v94\n\ts_waitcnt vmcnt(0)\n\ts_nop 7\n\t"
#define ATT_LOOP \
    "s_nop 4\n\tv_exp_f32 v18, v18\n\tv_exp_f32 v19, v19\n\tv_exp_f32 v20, v20\n\tv_mfma_f32_32x32x16_bf16 v[32:47], v[132:135], v[80:83], 0\n\tv_exp_f32 v21, v21\n\tv_exp_f32 v22, v22\n\t" \
    "v_exp_f32 v23, v23\n\tv_exp_f32 v24, v24\n\tv_exp_f32 v25, v25\n\tv_exp_f32 v26, v26\n\tv_smfmac_f32_32x32x32_bf16 v[48:63], v[160:163], v[16:23], v94\n\tv_exp_f32 v27, v27\n\t" \
    "v_exp_f32 v28, v28\n\tv_exp_f32 v29, v29\n\tv_exp_f32 v30, v30\n\tv_exp_f32 v31, v31\n\tv_exp_f32 v32, v32\n\tv_exp_f32 v33, v33\n\t" \
    "v_smfmac_f32_32x32x32_bf16 v[48:63], v[164:167], v[24:31], v94\n\tds_write_b128 v88, v[64:67] offset:18432\n\tds_write_b128 v88, v[68:71] offset:19456\n\t" \
    "ds_write_b32 v89, v72 offset:20480\n\tv_exp_f32 v34, v34\n\tv_exp_f32 v35, v35\n\tv_exp_f32 v36, v36\n\tv_mfma_f32_32x32x16_bf16 v[16:31], v[136:139], v[80:83], 0\n\tv_exp_f32 v37, v37\n\t" \
    "v_exp_f32 v38, v38\n\tv_exp_f32 v39, v39\n\tv_exp_f32 v40, v40\n\tv_exp_f32 v41, v41\n\tv_exp_f32 v42, v42\n\tv_smfmac_f32_32x32x32_bf16 v[48:63], v[168:171], v[32:39], v94\n\t" \
    "v_exp_f32 v43, v43\n\tv_exp_f32 v44, v44\n\tv_exp_f32 v45, v45\n\tv_exp_f32 v46, v46\n\tv_exp_f32 v47, v47\n\tv_exp_f32 v16, v16\n\tv_exp_f32 v17, v17\n\ts_mov_b32 m0, %[pd]\n\ts_nop 0\n\t" \
    "buffer_load_dwordx4 v95, %[rs], %[ps] offen offset:0 sc1 lds\n\tv_smfmac_f32_32x32x32_bf16 v[48:63], v[172:175], v[40:47], v94\n\tv_exp_f32 v18, v18\n\tv_exp_f32 v19, v19\n\t" \
    "v_exp_f32 v20, v20\n\tv_mfma_f32_32x32x16_bf16 v[32:47], v[140:143], v[80:83], 0\n\tv_exp_f32 v21, v21\n\tv_exp_f32 v22, v22\n\tv_exp_f32 v23, v23\n\tv_exp_f32 v24, v24\n\t" \
    "v_exp_f32 v25, v25\n\tv_exp_f32 v26, v26\n\tv_smfmac_f32_32x32x32_bf16 v[48:63], v[176:179], v[16:23], v94\n\tv_exp_f32 v27, v27\n\tv_exp_f32 v28, v28\n\tv_exp_f32 v29, v29\n\t" \
    "v_exp_f32 v30, v30\n\tv_exp_f32 v31, v31\n\tv_exp_f32 v32, v32\n\tv_exp_f32 v33, v33\n\tv_smfmac_f32_32x32x32_bf16 v[48:63], v[180:183], v[24:31], v94\n\tv_exp_f32 v34, v34\n\t" \
    "v_exp_f32 v35, v35\n\tv_exp_f32 v36, v36\n\tv_mfma_f32_32x32x16_bf16 v[16:31], v[144:147], v[80:83], 0\n\tv_exp_f32 v37, v37\n\tv_exp_f32 v38, v38\n\tv_exp_f32 v39, v39\n\t" \
    "v_exp_f32 v40, v40\n\tv_exp_f32 v41, v41\n\tv_exp_f32 v42, v42\n\tv_smfmac_f32_32x32x32_bf16 v[48:63], v[184:187], v[32:39], v94\n\tv_exp_f32 v43, v43\n\tv_exp_f32 v44, v44\n\t" \
    "v_exp_f32 v45, v45\n\tv_exp_f32 v46, v46\n\tv_exp_f32 v47, v47\n\tv_exp_f32 v16, v16\n\tv_exp_f32 v17, v17\n\ts_mov_b32 m0, %[wd]\n\ts_nop 0\n\t" \
    "buffer_load_dword v96, %[rs], %[ws] offen sc1 lds\n\tv_smfmac_f32_32x32x32_bf16 v[48:63], v[188:191], v[40:47], v94\n\ts_waitcnt lgkmcnt(0)\n\ts_barrier\n\t" \
    "ds_read_b128 v[64:67], v90 offset:18432\n\tds_read_b128 v[68:71], v90 offset:19456\n\tds_read_b32 v72, v91 offset:20480\n\ts_add_u32 %[qo], %[qo], 0x400\n\ts_and_b32 %[qo], %[qo], 0x1fff\n\t" \
    "v_add_u32 v97, %[qo], v98\n\tds_read_b128 v[84:87], v97\n\tv_exp_f32 v18, v18\n\tv_exp_f32 v19, v19\n\tv_exp_f32 v20, v20\n\tv_mfma_f32_32x32x16_bf16 v[32:47], v[148:151], v[80:83], 0\n\t" \
    "v_exp_f32 v21, v21\n\tv_exp_f32 v22, v22\n\tv_exp_f32 v23, v23\n\tv_exp_f32 v24, v24\n\tv_exp_f32 v25, v25\n\tv_exp_f32 v26, v26\n\t" \
    "v_smfmac_f32_32x32x32_bf16 v[48:63], v[192:195], v[16:23], v94\n\tv_exp_f32 v27, v27\n\tv_exp_f32 v28, v28\n\tv_exp_f32 v29, v29\n\tv_exp_f32 v30, v30\n\tv_exp_f32 v31, v31\n\t" \
    "v_exp_f32 v32, v32\n\tv_exp_f32 v33, v33\n\tv_smfmac_f32_32x32x32_bf16 v[48:63], v[196:199], v[24:31], v94\n\tv_exp_f32 v34, v34\n\tv_exp_f32 v35, v35\n\tv_exp_f32 v36, v36\n\t" \
    "v_mfma_f32_32x32x16_bf16 v[16:31], v[152:155], v[80:83], 0\n\tv_exp_f32 v37, v37\n\tv_exp_f32 v38, v38\n\tv_exp_f32 v39, v39\n\tv_exp_f32 v40, v40\n\tv_exp_f32 v41, v41\n\t" \
    "v_exp_f32 v42, v42\n\tv_smfmac_f32_32x32x32_bf16 v[48:63], v[200:203], v[32:39], v94\n\tv_exp_f32 v43, v43\n\tv_exp_f32 v44, v44\n\tv_exp_f32 v45, v45\n\tv_exp_f32 v46, v46\n\t" \
    "v_exp_f32 v47, v47\n\tv_exp_f32 v16, v16\n\tv_exp_f32 v17, v17\n\ts_mov_b32 m0, %[pd]\n\ts_nop 0\n\tbuffer_load_dwordx4 v95, %[rs], %[ps] offen offset:1024 sc1 lds\n\t" \
    "v_smfmac_f32_32x32x32_bf16 v[48:63], v[204:207], v[40:47], v94\n\tv_exp_f32 v18, v18\n\tv_exp_f32 v19, v19\n\tv_exp_f32 v20, v20\n\t" \
    "v_mfma_f32_32x32x16_bf16 v[32:47], v[156:159], v[80:83], 0\n\tv_exp_f32 v21, v21\n\tv_exp_f32 v22, v22\n\tv_exp_f32 v23, v23\n\tv_exp_f32 v24, v24\n\tv_exp_f32 v25, v25\n\t" \
    "v_exp_f32 v26, v26\n\tv_smfmac_f32_32x32x32_bf16 v[48:63], v[208:211], v[16:23], v94\n\tv_exp_f32 v27, v27\n\tv_exp_f32 v28, v28\n\tv_exp_f32 v29, v29\n\tv_exp_f32 v30, v30\n\t" \
    "v_exp_f32 v31, v31\n\tv_exp_f32 v32, v32\n\tv_exp_f32 v33, v33\n\tv_smfmac_f32_32x32x32_bf16 v[48:63], v[212:215], v[24:31], v94\n\tv_exp_f32 v34, v34\n\tv_exp_f32 v35, v35\n\t" \
    "v_exp_f32 v36, v36\n\ts_waitcnt lgkmcnt(0)\n\tv_mfma_f32_32x32x16_bf16 v[16:31], v[128:131], v[84:87], 0\n\tv_exp_f32 v37, v37\n\tv_exp_f32 v38, v38\n\tv_exp_f32 v39, v39\n\t" \
    "v_exp_f32 v40, v40\n\tv_exp_f32 v41, v41\n\tv_exp_f32 v42, v42\n\tv_smfmac_f32_32x32x32_bf16 v[48:63], v[216:219], v[32:39], v94\n\tv_exp_f32 v43, v43\n\tv_exp_f32 v44, v44\n\t" \
    "v_exp_f32 v45, v45\n\tv_exp_f32 v46, v46\n\tv_exp_f32 v47, v47\n\tv_exp_f32 v16, v16\n\tv_exp_f32 v17, v17\n\tv_smfmac_f32_32x32x32_bf16 v[48:63], v[220:223], v[40:47], v94\n\t" \
    "v_exp_f32 v18, v18\n\tv_exp_f32 v19, v19\n\tv_exp_f32 v20, v20\n\tv_mfma_f32_32x32x16_bf16 v[32:47], v[132:135], v[84:87], 0\n\tv_exp_f32 v21, v21\n\tv_exp_f32 v22, v22\n\t" \
    "v_exp_f32 v23, v23\n\tv_exp_f32 v24, v24\n\tv_exp_f32 v25, v25\n\tv_exp_f32 v26, v26\n\tv_smfmac_f32_32x32x32_bf16 v[64:79], v[160:163], v[16:23], v94\n\tv_exp_f32 v27, v27\n\t" \
    "v_exp_f32 v28, v28\n\tv_exp_f32 v29, v29\n\tv_exp_f32 v30, v30\n\tv_exp_f32 v31, v31\n\tv_exp_f32 v32, v32\n\tv_exp_f32 v33, v33\n\t" \
    "v_smfmac_f32_32x32x32_bf16 v[64:79], v[164:167], v[24:31], v94\n\tds_write_b128 v88, v[48:51] offset:0\n\tds_write_b128 v88, v[52:55] offset:1024\n\tds_write_b32 v89, v56 offset:2048\n\t" \
    "v_exp_f32 v34, v34\n\tv_exp_f32 v35, v35\n\tv_exp_f32 v36, v36\n\tv_mfma_f32_32x32x16_bf16 v[16:31], v[136:139], v[84:87], 0\n\tv_exp_f32 v37, v37\n\tv_exp_f32 v38, v38\n\t" \
    "v_exp_f32 v39, v39\n\tv_exp_f32 v40, v40\n\tv_exp_f32 v41, v41\n\tv_exp_f32 v42, v42\n\tv_smfmac_f32_32x32x32_bf16 v[64:79], v[168:171], v[32:39], v94\n\tv_exp_f32 v43, v43\n\t" \
    "v_exp_f32 v44, v44\n\tv_exp_f32 v45, v45\n\tv_exp_f32 v46, v46\n\tv_exp_f32 v47, v47\n\tv_exp_f32 v16, v16\n\tv_exp_f32 v17, v17\n\ts_mov_b32 m0, %[pd]\n\ts_nop 0\n\t" \
    "buffer_load_dwordx4 v95, %[rs], %[ps] offen offset:2048 sc1 lds\n\tv_smfmac_f32_32x32x32_bf16 v[64:79], v[172:175], v[40:47], v94\n\tv_exp_f32 v18, v18\n\tv_exp_f32 v19, v19\n\t" \
    "v_exp_f32 v20, v20\n\tv_mfma_f32_32x32x16_bf16 v[32:47], v[140:143], v[84:87], 0\n\tv_exp_f32 v21, v21\n\tv_exp_f32 v22, v22\n\tv_exp_f32 v23, v23\n\tv_exp_f32 v24, v24\n\t" \
    "v_exp_f32 v25, v25\n\tv_exp_f32 v26, v26\n\tv_smfmac_f32_32x32x32_bf16 v[64:79], v[176:179], v[16:23], v94\n\tv_exp_f32 v27, v27\n\tv_exp_f32 v28, v28\n\tv_exp_f32 v29, v29\n\t" \
    "v_exp_f32 v30, v30\n\tv_exp_f32 v31, v31\n\tv_exp_f32 v32, v32\n\tv_exp_f32 v33, v33\n\tv_smfmac_f32_32x32x32_bf16 v[64:79], v[180:183], v[24:31], v94\n\tv_exp_f32 v34, v34\n\t" \
    "v_exp_f32 v35, v35\n\tv_exp_f32 v36, v36\n\tv_mfma_f32_32x32x16_bf16 v[16:31], v[144:147], v[84:87], 0\n\tv_exp_f32 v37, v37\n\tv_exp_f32 v38, v38\n\tv_exp_f32 v39, v39\n\t" \
    "v_exp_f32 v40, v40\n\tv_exp_f32 v41, v41\n\tv_exp_f32 v42, v42\n\tv_smfmac_f32_32x32x32_bf16 v[64:79], v[184:187], v[32:39], v94\n\tv_exp_f32 v43, v43\n\tv_exp_f32 v44, v44\n\t" \
    "v_exp_f32 v45, v45\n\tv_exp_f32 v46, v46\n\tv_exp_f32 v47, v47\n\tv_exp_f32 v16, v16\n\tv_exp_f32 v17, v17\n\tv_smfmac_f32_32x32x32_bf16 v[64:79], v[188:191], v[40:47], v94\n\t" \
    "s_waitcnt lgkmcnt(0)\n\ts_barrier\n\tds_read_b128 v[48:51], v92 offset:0\n\tds_read_b128 v[52:55], v92 offset:1024\n\tds_read_b32 v56, v93 offset:2048\n\ts_add_u32 %[qo], %[qo], 0x400\n\t" \
    "s_and_b32 %[qo], %[qo], 0x1fff\n\tv_add_u32 v97, %[qo], v98\n\tds_read_b128 v[80:83], v97\n\tv_exp_f32 v18, v18\n\tv_exp_f32 v19, v19\n\tv_exp_f32 v20, v20\n\t" \
    "v_mfma_f32_32x32x16_bf16 v[32:47], v[148:151], v[84:87], 0\n\tv_exp_f32 v21, v21\n\tv_exp_f32 v22, v22\n\tv_exp_f32 v23, v23\n\tv_exp_f32 v24, v24\n\tv_exp_f32 v25, v25\n\t" \
    "v_exp_f32 v26, v26\n\tv_smfmac_f32_32x32x32_bf16 v[64:79], v[192:195], v[16:23], v94\n\tv_exp_f32 v27, v27\n\tv_exp_f32 v28, v28\n\tv_exp_f32 v29, v29\n\tv_exp_f32 v30, v30\n\t" \
    "v_exp_f32 v31, v31\n\tv_exp_f32 v32, v32\n\tv_exp_f32 v33, v33\n\tv_smfmac_f32_32x32x32_bf16 v[64:79], v[196:199], v[24:31], v94\n\tv_exp_f32 v34, v34\n\tv_exp_f32 v35, v35\n\t" \
    "v_exp_f32 v36, v36\n\tv_mfma_f32_32x32x16_bf16 v[16:31], v[152:155], v[84:87], 0\n\tv_exp_f32 v37, v37\n\tv_exp_f32 v38, v38\n\tv_exp_f32 v39, v39\n\tv_exp_f32 v40, v40\n\t" \
    "v_exp_f32 v41, v41\n\tv_exp_f32 v42, v42\n\tv_smfmac_f32_32x32x32_bf16 v[64:79], v[200:203], v[32:39], v94\n\tv_exp_f32 v43, v43\n\tv_exp_f32 v44, v44\n\tv_exp_f32 v45, v45\n\t" \
    "v_exp_f32 v46, v46\n\tv_exp_f32 v47, v47\n\tv_exp_f32 v16, v16\n\tv_exp_f32 v17, v17\n\ts_mov_b32 m0, %[pd]\n\ts_nop 0\n\tbuffer_load_dwordx4 v95, %[rs], %[ps] offen offset:3072 sc1 lds\n\t" \
    "v_smfmac_f32_32x32x32_bf16 v[64:79], v[204:207], v[40:47], v94\n\tv_exp_f32 v18, v18\n\tv_exp_f32 v19, v19\n\tv_exp_f32 v20, v20\n\t" \
    "v_mfma_f32_32x32x16_bf16 v[32:47], v[156:159], v[84:87], 0\n\tv_exp_f32 v21, v21\n\tv_exp_f32 v22, v22\n\tv_exp_f32 v23, v23\n\tv_exp_f32 v24, v24\n\tv_exp_f32 v25, v25\n\t" \
    "v_exp_f32 v26, v26\n\tv_smfmac_f32_32x32x32_bf16 v[64:79], v[208:211], v[16:23], v94\n\tv_exp_f32 v27, v27\n\tv_exp_f32 v28, v28\n\tv_exp_f32 v29, v29\n\tv_exp_f32 v30, v30\n\t" \
    "v_exp_f32 v31, v31\n\tv_exp_f32 v32, v32\n\tv_exp_f32 v33, v33\n\tv_smfmac_f32_32x32x32_bf16 v[64:79], v[212:215], v[24:31], v94\n\tv_exp_f32 v34, v34\n\tv_exp_f32 v35, v35\n\t" \
    "v_exp_f32 v36, v36\n\ts_waitcnt lgkmcnt(0)\n\tv_mfma_f32_32x32x16_bf16 v[16:31], v[128:131], v[80:83], 0\n\tv_exp_f32 v37, v37\n\tv_exp_f32 v38, v38\n\tv_exp_f32 v39, v39\n\t" \
    "v_exp_f32 v40, v40\n\tv_exp_f32 v41, v41\n\tv_exp_f32 v42, v42\n\tv_smfmac_f32_32x32x32_bf16 v[64:79], v[216:219], v[32:39], v94\n\tv_exp_f32 v43, v43\n\tv_exp_f32 v44, v44\n\t" \
    "v_exp_f32 v45, v45\n\tv_exp_f32 v46, v46\n\tv_exp_f32 v47, v47\n\tv_exp_f32 v16, v16\n\tv_exp_f32 v17, v17\n\tv_smfmac_f32_32x32x32_bf16 v[64:79], v[220:223], v[40:47], v94\n\ts_nop 15\n\t"

DI void attention_true_max(unsigned w, unsigned lane, const u32x32& kk, const u32x32& va, const u32x32& vb, f32x16& o) {
    auto kf = [&](int t) AI { return u32x4{kk[4 * t], kk[4 * t + 1], kk[4 * t + 2], kk[4 * t + 3]}; };
    auto vf = [&](int i) AI { const u32x32& v = i < 8 ? va : vb; const int b = 4 * (i & 7); return u32x4{v[b], v[b + 1], v[b + 2], v[b + 3]}; };
#pragma unroll 1
    for (unsigned j = 0; j < 8; ++j) {
        const u32x4 qf = lds_ld<u32x4>(L_Q + j * 1024 + lane * 16);
        float m = -__builtin_inff();
#pragma unroll
        for (int t = 0; t < 8; ++t) {
            const f32x16 s = mfma32(kf(t), qf, f32x16{});
#pragma unroll
            for (int e = 0; e < 16; ++e) m = fmaxf(m, s[e]);
        }
        m = swap32_max(m);
        if (lane < 32) lds_st<float>(L_MX + ((w * 8 + j) * 32 + lane) * 4, m);
    }
    bar_lds();
#pragma unroll 1
    for (unsigned j = 0; j < 8; ++j) {
        const u32x4 qf = lds_ld<u32x4>(L_Q + j * 1024 + lane * 16);
        float m = -__builtin_inff();
#pragma unroll
        for (int i = 0; i < 8; ++i) m = fmaxf(m, lds_ld<float>(L_MX + ((i * 8 + j) * 32 + (lane & 31)) * 4));
        f32x16 ci, acc = {};
#pragma unroll
        for (int e = 0; e < 16; ++e) ci[e] = -m;
#pragma unroll
        for (int t = 0; t < 8; ++t) {
            f32x16 s = mfma32(kf(t), qf, ci);
#pragma unroll
            for (int e = 0; e < 16; ++e) s[e] = __builtin_amdgcn_exp2f(s[e]);
            acc = smfmac(vf(2 * t), __builtin_shufflevector(s, s, 0, 1, 2, 3, 4, 5, 6, 7), acc, 0xDDDD);
            acc = smfmac(vf(2 * t + 1), __builtin_shufflevector(s, s, 8, 9, 10, 11, 12, 13, 14, 15), acc, 0xDDDD);
        }
#pragma unroll
        for (int e = 0; e < 9; ++e) lds_st<float>(L_CH + w * 2304 + e * 256 + lane * 4, acc[e]);
        bar_lds();
        if (j == ((w + 7) & 7)) {
#pragma unroll
            for (int e = 0; e < 9; ++e) {
                float s = 0.f;
#pragma unroll
                for (int i = 0; i < 8; ++i) s += lds_ld<float>(L_CH + i * 2304 + e * 256 + lane * 4);
                o[e] = s;
            }
        }
        bar_lds();
    }
}

DI void attention(const Ctx& c0, unsigned layer) {
    const Ctx c = fresh(c0); const unsigned lane = lane_id();
    const unsigned w = c.w, head = 4 * c.g() + (c.r() & 3), qb = c.r() >> 2, t = lane & 31, h = lane >> 5;
    u32x32 kk, va, vb;
    f32x16 oa, ob, sa, sb;
    u32x4 qa, qn;
    u32x8 fw;
    asm volatile(ATT_FILL : "={v[160:191]}"(va), "={v[192:223]}"(vb), "={v[48:63]}"(oa), "={v[64:79]}"(ob) : [cv] "v"((t == 16 || t == 20) ? ONES : 0u));
    if (w == 0 && lane == 0) lds_st<unsigned>(L_VOTE, 0u);
    const unsigned l16 = lane * 16, l4 = lane * 4;
    const unsigned wr16 = L_CH + w * 2304 + l16, wr4 = L_CH + w * 2304 + l4;
    const unsigned rd16 = L_CH + ((w + 2) & 7) * 2304 + l16, rd4 = L_CH + ((w + 2) & 7) * 2304 + l4;
    const unsigned hso = layer * 2 * MB + head * 65536;
    unsigned sk = OFF_K + hso + w * 8192, sv = OFF_V + hso + w * 8192, qo = w * 1024, qtmp;
    wait_flags(c.ws, OFF_FQKV + layer * 8192 + (head >> 1) * 512 + ((lane & 8) ? 8 * qb + w : 8 * w + (lane & 7)) * 8, c.tag);
    STAMP(16 + layer * 16);
    asm volatile(ATT_FIRST
                 : "={v[128:159]}"(kk), "+{v[160:191]}"(va), "+{v[192:223]}"(vb), "+{v[48:63]}"(oa), "+{v[64:79]}"(ob), "={v[16:31]}"(sa), "={v[32:47]}"(sb),
                   "={v[80:83]}"(qa), "={v[84:87]}"(qn), "={v97}"(qtmp), "={v[100:107]}"(fw), [qo] "+s"(qo), [sk] "+s"(sk), [sv] "+s"(sv)
                 : "{v88}"(wr16), "{v89}"(wr4), "{v90}"(rd16), "{v91}"(rd4), "{v92}"(rd16), "{v93}"(rd4), "{v94}"(0xDDDD), "{v95}"(l16), "{v96}"(h * 256 + t * 16),
                   "{v98}"(L_Q + l16), "{v108}"(lane * 8), [rs] "s"(c.rws), [sq] "s"(OFF_Q + hso + (8 * qb + w) * 1024), [qd] "s"(L_Q + w * 1024), [sfw] "s"(OFF_FW),
                   [vm] "s"(0x0000ffff0000ffffull)
                 : "m0", "scc", "memory");
    STAMP(17 + layer * 16);
    if (layer == 0) {
        const unsigned tl = (unsigned)c.tag, th = (unsigned)(c.tag >> 32);
        if (!__all(fw[0] == tl && fw[1] == th && fw[2] == tl && fw[3] == th && fw[4] == tl && fw[5] == th && fw[6] == tl && fw[7] == th)) {
#pragma unroll 1
            for (int q = 0; q < 4; ++q) wait_flags(c.ws, OFF_FW + q * 512 + lane * 8, c.tag);
        }
        asm volatile("" ::: "memory");
    }
#pragma unroll 1
    for (unsigned part = 0; part < 3; ++part) {
        const unsigned src = L_CH + ((w + (part == 2 ? 1 : 2)) & 7) * 2304, wi = c.r() * 8 + w;
        const unsigned ps = part < 2 ? OFF_WO + c.fp() * 65536 + w * 8192 + part * 4096 : OFF_WG + c.col8() * 229376 + w * 4096;
        const unsigned pd = part < 2 ? w * 8192 + part * 4096 : 2 * L_SLOT + w * 4096;
        const unsigned wsrc = part < 2 ? OFF_WG + (c.b & 1) * (8 * 229376) + wi * 7168 + part * 3072 : OFF_WD + (c.b & 1) * (4 * 229376) + wi * 3584;
        asm volatile(ATT_LOOP
                     : "+{v[48:63]}"(oa), "+{v[64:79]}"(ob), "+{v[16:31]}"(sa), "={v[32:47]}"(sb), "+{v[80:83]}"(qa), "={v[84:87]}"(qn), "={v97}"(qtmp), [qo] "+s"(qo)
                     : "{v[128:159]}"(kk), "{v[160:191]}"(va), "{v[192:223]}"(vb), "{v88}"(wr16), "{v89}"(wr4), "{v90}"(rd16), "{v91}"(rd4), "{v92}"(src + l16), "{v93}"(src + l4),
                       "{v94}"(0xDDDD), "{v95}"(l16), "{v96}"(lane * 64), "{v98}"(L_Q + l16), [rs] "s"(c.rws), [ps] "s"(ps), [pd] "s"(pd), [ws] "s"(wsrc), [wd] "s"(L_SINK)
                     : "m0", "scc", "memory");
    }
    STAMP(18 + layer * 16);
    f32x16 o;
#pragma unroll
    for (int e = 0; e < 9; ++e) o[e] = oa[e] + ob[e];
    const float mag = fabsf(o[0]) + fabsf(o[1]) + fabsf(o[2]) + fabsf(o[3]) + fabsf(o[4]) + fabsf(o[5]) + fabsf(o[6]) + fabsf(o[7]) + o[8];
    const bool ok = (o[8] >= 9.313225746e-10f) && (mag < __builtin_inff());
    if (!ok) lds_st<unsigned>(L_VOTE, 1u);
    const unsigned tt = 8 * qb + ((w + 7) & 7);
    auto store_o = [&]() AI {
        const float inv = 1.0f / o[8];
        st16<SC1>(u32x4{pk2(o[0] * inv, o[1] * inv), pk2(o[2] * inv, o[3] * inv), pk2(o[4] * inv, o[5] * inv), pk2(o[6] * inv, o[7] * inv)},
                  c.rws, (t >> 4) * 1024 + (t & 15) * 16 + h * 256, OFF_O + layer * 2 * MB + tt * 32768 + (head >> 1) * 2048 + (head & 1) * 512);
    };
    store_o();
    bar_lds();
    if (rfl(lds_ld<unsigned>(L_VOTE))) {
        attention_true_max(w, lane, kk, va, vb, o);
        vmwait<0>();
        store_o();
    }
    set_flag(c.ws, OFF_FO + layer * 16384 + tt * 256 + head * 8, c.tag, lane);
}

DI void chain_help(const Ctx& c0, unsigned layer) {
    const Ctx c = fresh(c0); const unsigned lane = lane_id();
    bar(); issue_chunk<3>(c, lane);
    if (layer == 0 && c.w == 4 && lane < 56) {
        const unsigned vo = (lane >= 28 ? 7168 - 448 : 0) + lane * 16, so = OFF_CSG + c.col8() * 448;
        f32x4 s = {};
#pragma unroll
        for (int kb = 0; kb < 16; ++kb) s += bc<f32x4>(ld16<SC1>(c.rws, vo, so + kb * 14336));
        lds_st<f32x4>(L_CSG + lane * 16, s);
    }
    bar_lds();
    bar();
    vmwait<0>(); bar(); issue_chunk<4>(c, lane); issue_chunk<5>(c, lane);
    if (layer == 0) {
        const unsigned part = c.r() * 4 + (c.w - 4), vo = (lane >> 3) * 1024 + (lane & 7) * 64, so = part * 12288 + (c.b & 1) * 512;
        warm(c.rwqkv, vo, so); warm(c.rwqkv, vo, so + 4096);
        vmwait<9>();
    } else vmwait<7>();
    bar(); issue_chunk<6>(c, lane);
    vmwait<7>(); bar(); issue_chunk<7>(c, lane);
    vmwait<7>(); bar(); issue_chunk<8>(c, lane);
    vmwait<7>(); bar(); issue_chunk<9>(c, lane);
    vmwait<7>(); bar(); issue_chunk<10>(c, lane);
    vmwait<8>(); bar(); issue_chunk<11>(c, lane);
    bar(); issue_chunk<12>(c, lane);
    bar();
    vmwait<8>(); bar(); issue_chunk<13>(c, lane);
    vmwait<8>(); bar(); issue_chunk<14>(c, lane);
    vmwait<8>(); bar(); issue_chunk<15>(c, lane);
    vmwait<8>(); bar(); issue_chunk<16>(c, lane);
    if (layer == 0) {
        const unsigned vw = qkv_w_voff<false>(lane), hw = c.w - 4;
        auto wpart = [&](int i) AI {
#pragma unroll
            for (int e = 0; e < 8; ++e) { const unsigned p = 32 * i + 8 * hw + e; qkv_w_piece<false>(c, vw, p / 12, p % 12, L_SLOT + p * 1024); }
        };
        vmwait<8>(); bar(); wpart(0);
        vmwait<8>(); bar(); wpart(1);
        bar(); wpart(2);
        bar();
        vmwait<0>(); bar();
        bar();
    } else {
        vmwait<8>(); bar();
        vmwait<0>(); bar();
        bar();
        bar();
    }
}

template <int MODE> DI void proj(const Ctx& c0, unsigned layer, void* out) {
    const Ctx c = fresh(c0); const unsigned lane = lane_id();
    constexpr int KS = MODE ? 28 : 8, N0 = MODE ? 10 : 0, TR = MODE ? 14 : 8, R = 5, P = 4;
    const unsigned w = c.w, kh = w >> 1, tt = c.pair() + (w & 1), ft = 2 * c.fp() + kh;
    const unsigned cc = lane & 15, q = lane >> 4, vo = lane * 16;
    u32x4 res[2];
    if (MODE == 0 && layer == 0) {
#pragma unroll
        for (int a = 0; a < 2; ++a) {
            const u32x2 v0 = ld8<0>(c.rx, cc * 1024 + q * 8, tt * 32768 + a * 16384 + ft * 64), v1 = ld8<0>(c.rx, cc * 1024 + q * 8 + 32, tt * 32768 + a * 16384 + ft * 64);
            res[a] = u32x4{v0.x, v0.y, v1.x, v1.y};
        }
    } else {
#pragma unroll
        for (int a = 0; a < 2; ++a) res[a] = ldfrag(c.rws, vo, (MODE ? OFF_X1 + layer * 2 * MB : OFF_X2) + tt * 32768 + ft * 2048, a);
    }
    if (MODE) wait_flags(c.ws, OFF_FH + layer * 8192 + tt * 128 + kh * 64 + (lane & 7) * 8, c.tag);
    else wait_flags(c.ws, OFF_FO + layer * 16384 + tt * 256 + kh * 128 + (lane & 15) * 8, c.tag);
    STAMP((MODE ? 24 : 19) + layer * 16);
    const unsigned tso = MODE ? OFF_H + layer * 7 * MB + (tt * 56 + 28 * kh) * 2048 : OFF_O + layer * 2 * MB + (tt * 16 + 8 * kh) * 2048;
    auto tload = [&](int s, int a) AI {
        if (MODE && s % 7 == 6) {
            const int i = 2 * s + a;
            const u32x2 lo = ld8<SC1>(c.rws, lane * 8 + (i & 3) * 1024, tso + (i >> 2) * 4096), hi = ld8<SC1>(c.rws, lane * 8 + (i & 3) * 1024 + 512, tso + (i >> 2) * 4096);
            return u32x4{lo.x, lo.y, hi.x, hi.y};
        }
        return ldfrag(c.rws, vo, tso, 2 * s + a);
    };
    u32x4 tok[TR][2], wf[R];
#pragma unroll
    for (int s = 0; s < TR; ++s) { tok[s][0] = tload(s, 0); tok[s][1] = tload(s, 1); }
    bar();
    unsigned wa[4] = {vo + kh * 6144, vo + kh * 2048 + 2048, vo + kh * 6144 + 2 * L_SLOT, vo + kh * 2048 + 2048 + 2 * L_SLOT};
    PIN(wa[0]); PIN(wa[1]); PIN(wa[2]); PIN(wa[3]);
    auto wread = [&](int n) AI { const int s = n >> 2, sl = (N0 + (s >> 2)) & 3; return lds_ld<u32x4>(wa[((n >> 1) & 1) + (sl & 2)] + (sl & 1) * L_SLOT + (s & 3) * 8192 + (n & 1) * 1024); };
#pragma unroll
    for (int n = 0; n < P; ++n) wf[n] = wread(n);
    f32x4 acc[4][2] = {};
    static_for<0, 4 * KS>([&](auto ic) AI {
        constexpr int n = decltype(ic)::v, s = n >> 2, i = n & 3;
        if constexpr (n + P < 4 * KS) wf[(n + P) % R] = wread(n + P);
        acc[i][0] = mfma16(wf[n % R], tok[s % TR][0], acc[i][0]); PIN(acc[i][0]);
        acc[i][1] = mfma16(wf[n % R], tok[s % TR][1], acc[i][1]); PIN(acc[i][1]);
        if constexpr (i == 3 && s + TR < KS) { tok[s % TR][0] = tload(s + TR, 0); tok[s % TR][1] = tload(s + TR, 1); }
        SB();
        if constexpr ((n & 15) == 15 && n + 1 < 4 * KS) { bar(); if (MODE == 1) STAMP(56 + n / 16); }
    });
    STAMP((MODE ? 25 : 20) + layer * 16);
#pragma unroll
    for (int i = 0; i < 4; ++i) lds_st<f32x4>(L_EX + w * 4096 + i * 1024 + vo, acc[2 + (i >> 1)][i & 1]);
    bar_lds();
    u32x2 p[2][2];
    float s1[2], s2[2];
#pragma unroll
    for (int a = 0; a < 2; ++a) {
        s1[a] = 0.f; s2[a] = 0.f;
#pragma unroll
        for (int i = 0; i < 2; ++i) {
            const f32x4 y = acc[i][a] + lds_ld<f32x4>(L_EX + (w ^ 2) * 4096 + (2 * i + a) * 1024 + vo) +
                            f32x4{bflo(res[a][2 * i]), bfhi(res[a][2 * i]), bflo(res[a][2 * i + 1]), bfhi(res[a][2 * i + 1])};
            p[a][i] = u32x2{pk2(y[0], y[1]), pk2(y[2], y[3])};
            s1[a] += (y[0] + y[1]) + (y[2] + y[3]);
            s2[a] = fmaf(y[0], y[0], fmaf(y[1], y[1], fmaf(y[2], y[2], fmaf(y[3], y[3], s2[a]))));
        }
    }
    if (MODE == 1 && layer == 1) {
        const rsrc_t ro = mkrsrc(out, SEQ * DM * 2);
#pragma unroll
        for (int a = 0; a < 2; ++a) st16<0>(merge16(p[a][0], p[a][1]), ro, cc * 1024 + (q & 1) * 32 + (q >> 1) * 16, tt * 32768 + a * 16384 + ft * 64);
        return;
    }
#pragma unroll
    for (int a = 0; a < 2; ++a) stfrag(u32x4{p[a][0].x, p[a][0].y, p[a][1].x, p[a][1].y}, c.rws, vo, (MODE ? OFF_X2 : OFF_X1 + layer * 2 * MB) + tt * 32768 + ft * 2048, a);
    const u32x2 x1 = swap16(bc<unsigned>(s1[0]), bc<unsigned>(s1[1])), x2 = swap16(bc<unsigned>(s2[0]), bc<unsigned>(s2[1]));
    const float t1 = swap32_add(bc<float>(x1.x) + bc<float>(x1.y)), t2 = swap32_add(bc<float>(x2.x) + bc<float>(x2.y));
    if (lane < 32) st8<SC1>(u32x2{bc<unsigned>(t1), bc<unsigned>(t2)}, c.rws, lane * 8, (MODE ? OFF_ST2 : OFF_ST1 + layer * 262144) + tt * 4096 + ft * 256);
    set_flag(c.ws, (MODE ? OFF_FX2 : OFF_FX1 + layer * 8192) + tt * 128 + ft * 8, c.tag, lane);
}

DI void gateup(const Ctx& c0, unsigned layer) {
    const Ctx c = fresh(c0); const unsigned lane = lane_id();
    constexpr int TR = 4, R = 6, P = 5;
    const unsigned w = c.w, cj = c.col8(), tt = c.quad() + w, q = lane >> 4, vo = lane * 16;
    wait_flags(c.ws, OFF_FX1 + layer * 8192 + tt * 128 + (lane & 15) * 8, c.tag);
    STAMP(22 + layer * 16);
    const unsigned tso = OFF_X1 + layer * 2 * MB + tt * 32768;
    u32x4 tok[TR][2], wf[R];
    u32x2 sp[8];
#pragma unroll
    for (int s = 0; s < 2 * TR; ++s) tok[s >> 1][s & 1] = ldfrag(c.rws, vo, tso, s);
    load_stats(c, OFF_ST1 + layer * 262144, tt, lane, sp);
    bar();
    unsigned vh = vo + 2 * L_SLOT;
    PIN(vh);
    auto wread = [&](int n) AI { const int sl = (2 + n / 28) & 3; return lds_ld<u32x4>((sl & 2 ? vh : vo) + (sl & 1) * L_SLOT + (n % 28) * 1024); };
#pragma unroll
    for (int n = 0; n < P; ++n) wf[n] = wread(n);
    f32x4 acc[14][2] = {};
    float mu, rs;
    static_for<0, 224>([&](auto ic) AI {
        constexpr int n = decltype(ic)::v, k = n / 14, i = n % 14;
        if constexpr (n + P < 224) wf[(n + P) % R] = wread(n + P);
        acc[i][0] = mfma16(wf[n % R], tok[k % TR][0], acc[i][0]); PIN(acc[i][0]);
        acc[i][1] = mfma16(wf[n % R], tok[k % TR][1], acc[i][1]); PIN(acc[i][1]);
        if constexpr (i == 13 && k + TR < 16) { tok[k % TR][0] = ldfrag(c.rws, vo, tso, 2 * (k + TR)); tok[k % TR][1] = ldfrag(c.rws, vo, tso, 2 * (k + TR) + 1); }
        if constexpr (n == 20) { reduce_stats(sp, mu, rs); asm volatile("" : "+v"(mu), "+v"(rs)); }
        SB();
        if constexpr (n % 28 == 27) { bar(); STAMP(48 + n / 28); }
    });
    STAMP(23 + layer * 16);
    const u32x2 mu2 = swap16(bc<unsigned>(mu), bc<unsigned>(mu)), rr2 = swap16(bc<unsigned>(rs * rs), bc<unsigned>(rs * rs));
    const unsigned ho = OFF_H + layer * 7 * MB + (tt * 56 + 7 * (cj >> 1)) * 2048;
    auto hval = [&](int i, int a) AI {
        const float m = bc<float>(mu2[a]);
        const f32x4 cg = lds_ld<f32x4>(L_CSG + (16 * i + 4 * q) * 4), cu = lds_ld<f32x4>(L_CSG + (112 + 16 * i + 4 * q) * 4);
        const f32x4 y = (acc[2 * i][a] - m * cg) * bc<float>(rr2[a]) * (acc[2 * i + 1][a] - m * cu);
        return u32x2{pk2(y[0], y[1]), pk2(y[2], y[3])};
    };
#pragma unroll
    for (int e = 0; e < 3; ++e)
#pragma unroll
        for (int a = 0; a < 2; ++a) {
            const u32x2 lo = hval(2 * e, a), hi = hval(2 * e + 1, a);
            stfrag(u32x4{lo.x, lo.y, hi.x, hi.y}, c.rws, vo, ho + (cj & 1) * 6144, 2 * e + a);
        }
#pragma unroll
    for (int a = 0; a < 2; ++a) st8<SC1>(hval(6, a), c.rws, lane * 8 + a * 1024, ho + 12288 + (cj & 1) * 512);
    set_flag(c.ws, OFF_FH + layer * 8192 + tt * 128 + cj * 8, c.tag, lane);
}

__global__ void __launch_bounds__(512, 2) fused_layers(const void* x, const void* wqkv, const void* wo, const void* wg, const void* wu, const void* wd, void* out, char* ws) {
    __shared__ __attribute__((aligned(16))) char lds[LDS_BYTES];
    asm volatile("" ::"v"(lds) : "memory");
    Ctx c;
    c.ws = ws; c.b = blockIdx.x; c.w = rfl(threadIdx.x >> 6);
    c.rws = mkrsrc(ws, WS_USED); c.rx = mkrsrc(x, SEQ * DM * 2); c.rwqkv = mkrsrc(wqkv, DM * 3 * DM * 2);
    STAMP(0);
    if (c.w < 4) {
        const u64 e = __hip_atomic_load((gu64*)(ws + OFF_EPOCH), RLX_AGENT) + 1;
        c.tag = ((u64)rfl((unsigned)(e >> 32)) << 32) | rfl((unsigned)e);
        if (c.w == 0 && lane_id() == 0) lds_st<u64>(L_VOTE + 8, c.tag);
        asm volatile("s_waitcnt lgkmcnt(0)" ::: "memory");
        phaseA_mfma(c);
    } else {
        phaseA_help(c, wo, wg, wu, wd);
        const u64 e = lds_ld<u64>(L_VOTE + 8);
        c.tag = ((u64)rfl((unsigned)(e >> 32)) << 32) | rfl((unsigned)e);
    }
    STAMP(1);
#pragma unroll 1
    for (unsigned layer = 0; layer < 2; ++layer) {
        attention(c, layer);
        STAMP(2 + layer * 8);
        if (c.w < 4) {
            proj<0>(c, layer, out);
            STAMP(3 + layer * 8);
            gateup(c, layer);
            STAMP(4 + layer * 8);
            proj<1>(c, layer, out);
            STAMP(5 + layer * 8);
            if (layer == 0) phaseF(c);
            STAMP(6 + layer * 8);
        } else {
            chain_help(c, layer);
        }
    }
    STAMP_OUT();
    if (c.b == 0 && c.w == 0 && lane_id() == 0) __hip_atomic_store((gu64*)(ws + OFF_EPOCH), c.tag, RLX_AGENT);
}

}

extern "C" void kernel_launch(void* const* d_in, const int* in_sizes, int n_in, void* d_out, int out_size, void* d_ws, size_t ws_size, hipStream_t stream) {
    (void)in_sizes; (void)n_in; (void)out_size; (void)ws_size;
    fused_layers<<<256, 512, 0, stream>>>(d_in[0], d_in[1], d_in[2], d_in[4], d_in[5], d_in[6], d_out, (char*)d_ws);
}
```

```cpp
#include <hip/hip_runtime.h>
#include <hip/hip_bf16.h>
#include <stdint.h>

#pragma clang diagnostic ignored "-Winline-asm"

typedef __attribute__((ext_vector_type(2))) unsigned u32x2;
typedef __attribute__((ext_vector_type(4))) unsigned u32x4;
typedef __attribute__((ext_vector_type(8))) unsigned u32x8;
typedef __attribute__((ext_vector_type(32))) unsigned u32x32;
typedef __attribute__((ext_vector_type(4))) float f32x4;
typedef __attribute__((ext_vector_type(8))) float f32x8;
typedef __attribute__((ext_vector_type(16))) float f32x16;
typedef __attribute__((ext_vector_type(4))) short s16x4;
typedef __bf16 bf16x2 __attribute__((ext_vector_type(2)));
typedef __bf16 bf16x8 __attribute__((ext_vector_type(8)));
typedef __bf16 bf16x16 __attribute__((ext_vector_type(16)));
typedef __amdgpu_buffer_rsrc_t rsrc_t;
typedef unsigned long long u64;
typedef __attribute__((address_space(1))) u64 gu64;
#define LAS __attribute__((address_space(3)))
#define DI __device__ __forceinline__
#define AI __attribute__((always_inline))
#define SB() __builtin_amdgcn_sched_barrier(0)
#define PIN(x) asm volatile("" : "+v"(x))
#define RLX_AGENT __ATOMIC_RELAXED, __HIP_MEMORY_SCOPE_AGENT

namespace {

constexpr int SEQ = 2048, DM = 512, FF = 1792;
constexpr int SC1 = 16;
constexpr unsigned ONES = 0x3f803f80u;

constexpr unsigned MB = 1u << 20;
constexpr unsigned OFF_EPOCH = 0;
constexpr unsigned OFF_FQKV = 4096;
constexpr unsigned OFF_FO = OFF_FQKV + 2 * 16 * 64 * 8;
constexpr unsigned OFF_FX1 = OFF_FO + 2 * 64 * 32 * 8;
constexpr unsigned OFF_FH = OFF_FX1 + 2 * 64 * 16 * 8;
constexpr unsigned OFF_FX2 = OFF_FH + 2 * 64 * 16 * 8;
constexpr unsigned OFF_FW = OFF_FX2 + 64 * 16 * 8;
constexpr unsigned OFF_CSG = 128 * 1024;
constexpr unsigned OFF_ST1 = 512 * 1024;
constexpr unsigned OFF_ST2 = OFF_ST1 + 2 * 256 * 1024;
constexpr unsigned OFF_WO = OFF_ST2 + 256 * 1024;
constexpr unsigned OFF_WD = OFF_WO + 512 * 1024;
constexpr unsigned OFF_WG = OFF_WD + 1792 * 1024;
constexpr unsigned OFF_Q = 8 * MB;
constexpr unsigned OFF_K = OFF_Q + 4 * MB;
constexpr unsigned OFF_V = OFF_K + 4 * MB;
constexpr unsigned OFF_O = OFF_V + 4 * MB;
constexpr unsigned OFF_X1 = OFF_O + 4 * MB;
constexpr unsigned OFF_X2 = OFF_X1 + 4 * MB;
constexpr unsigned OFF_H = OFF_X2 + 2 * MB;
constexpr unsigned OFF_DBG = OFF_H + 14 * MB;
constexpr unsigned WS_USED = OFF_DBG + 2 * MB;
static_assert(OFF_WG + 3584 * 1024 <= OFF_Q, "workspace map");

constexpr unsigned L_SLOT = 32768;
constexpr unsigned L_Q = 98304;
constexpr unsigned L_CH = L_Q + 8192;
constexpr unsigned L_EX = 131072;
constexpr unsigned L_CSQ = 147456;
constexpr unsigned L_MU = L_CSQ + 1024;
constexpr unsigned L_RS = L_MU + 512;
constexpr unsigned L_CSG = L_RS + 512;
constexpr unsigned L_VOTE = L_CSG + 1024;
constexpr unsigned L_SINK = L_VOTE + 256;
constexpr unsigned L_MX = L_SINK + 256;
constexpr unsigned LDS_BYTES = 163840;
static_assert(L_CH + 2 * 8 * 2304 <= L_CSQ && L_MX + 8192 <= LDS_BYTES, "LDS map");

template <int I> struct IC { static constexpr int v = I; };
template <int B, int E, class Fn> DI void static_for(Fn&& f) {
    if constexpr (E - B == 1) f(IC<B>{});
    else if constexpr (E - B > 1) { static_for<B, (B + E) / 2>(f); static_for<(B + E) / 2, E>(f); }
}
template <class T, class U> DI T bc(U v) { return __builtin_bit_cast(T, v); }
DI unsigned rfl(unsigned v) { return __builtin_amdgcn_readfirstlane(v); }
DI unsigned lane_id() { unsigned l; asm volatile("v_mbcnt_lo_u32_b32 %0, -1, 0\n\tv_mbcnt_hi_u32_b32 %0, -1, %0" : "=v"(l)); return l; }
DI rsrc_t mkrsrc(const void* p, unsigned bytes) { return __builtin_amdgcn_make_buffer_rsrc((void*)p, (short)0, (int)bytes, 0x00020000); }
DI f32x16 mfma32(u32x4 a, u32x4 b, f32x16 c) { return __builtin_amdgcn_mfma_f32_32x32x16_bf16(bc<bf16x8>(a), bc<bf16x8>(b), c, 0, 0, 0); }
DI f32x4 mfma16(u32x4 a, u32x4 b, f32x4 c) { return __builtin_amdgcn_mfma_f32_16x16x32_bf16(bc<bf16x8>(a), bc<bf16x8>(b), c, 0, 0, 0); }
DI f32x16 smfmac(u32x4 a, f32x8 b, f32x16 c, int idx) { return __builtin_amdgcn_smfmac_f32_32x32x32_bf16(bc<bf16x8>(a), bc<bf16x16>(b), c, idx, 0, 0); }
DI unsigned pk2(float lo, float hi) { bf16x2 v = {(__bf16)lo, (__bf16)hi}; return bc<unsigned>(v); }
DI float bflo(unsigned d) { return bc<float>(d << 16); }
DI float bfhi(unsigned d) { return bc<float>(d & 0xffff0000u); }
DI u32x2 swap32(unsigned a, unsigned b) { auto r = __builtin_amdgcn_permlane32_swap(a, b, false, false); return u32x2{r[0], r[1]}; }
DI u32x2 swap16(unsigned a, unsigned b) { auto r = __builtin_amdgcn_permlane16_swap(a, b, false, false); return u32x2{r[0], r[1]}; }
DI float swap32_add(float v) { const u32x2 r = swap32(bc<unsigned>(v), bc<unsigned>(v)); return bc<float>(r.x) + bc<float>(r.y); }
DI float swap32_max(float v) { const u32x2 r = swap32(bc<unsigned>(v), bc<unsigned>(v)); return fmaxf(bc<float>(r.x), bc<float>(r.y)); }
DI u32x4 merge16(u32x2 a, u32x2 b) { const u32x2 x = swap16(a.x, b.x), y = swap16(a.y, b.y); return u32x4{x.x, y.x, x.y, y.y}; }

template <class T> DI T lds_ld(unsigned a) { return *(const LAS T*)(size_t)a; }
template <class T> DI void lds_st(unsigned a, T v) { *(LAS T*)(size_t)a = v; }
DI u32x4 lds_tr(unsigned a, unsigned step) {
    const u32x2 lo = bc<u32x2>(__builtin_amdgcn_ds_read_tr16_b64_v4i16((LAS s16x4*)(size_t)a));
    const u32x2 hi = bc<u32x2>(__builtin_amdgcn_ds_read_tr16_b64_v4i16((LAS s16x4*)(size_t)(a + step)));
    return u32x4{lo.x, lo.y, hi.x, hi.y};
}

template <int AUX> DI u32x4 ld16(rsrc_t r, unsigned vo, unsigned so) { return __builtin_amdgcn_raw_buffer_load_b128(r, vo, so, AUX); }
template <int AUX> DI u32x2 ld8(rsrc_t r, unsigned vo, unsigned so) { return __builtin_amdgcn_raw_buffer_load_b64(r, vo, so, AUX); }
template <int AUX> DI void st16(u32x4 v, rsrc_t r, unsigned vo, unsigned so) { __builtin_amdgcn_raw_buffer_store_b128(v, r, vo, so, AUX); }
template <int AUX> DI void st8(u32x2 v, rsrc_t r, unsigned vo, unsigned so) { __builtin_amdgcn_raw_buffer_store_b64(v, r, vo, so, AUX); }
DI u32x4 ldfrag(rsrc_t r, unsigned vo, unsigned so, int i) { return ld16<SC1>(r, vo + (i & 3) * 1024, so + (i >> 2) * 4096); }
DI void stfrag(u32x4 v, rsrc_t r, unsigned vo, unsigned so, int i) { st16<SC1>(v, r, vo + (i & 3) * 1024, so + (i >> 2) * 4096); }

template <bool DEV, int IMM> DI void dma(rsrc_t r, unsigned vo, unsigned so, unsigned la) {
    if constexpr (DEV) asm volatile("s_mov_b32 m0, %0\n\ts_nop 0\n\tbuffer_load_dwordx4 %1, %2, %3 offen offset:%4 sc1 lds" ::"s"(la), "v"(vo), "s"(r), "s"(so), "i"(IMM) : "m0", "memory");
    else asm volatile("s_mov_b32 m0, %0\n\ts_nop 0\n\tbuffer_load_dwordx4 %1, %2, %3 offen offset:%4 lds" ::"s"(la), "v"(vo), "s"(r), "s"(so), "i"(IMM) : "m0", "memory");
}
DI void warm(rsrc_t r, unsigned vo, unsigned so) {
    asm volatile("s_mov_b32 m0, %0\n\ts_nop 0\n\tbuffer_load_dword %1, %2, %3 offen sc1 lds" ::"s"(L_SINK), "v"(vo), "s"(r), "s"(so) : "m0", "memory");
}
template <int N> DI void vmwait() { asm volatile("s_waitcnt vmcnt(%0)" ::"i"(N) : "memory"); }
DI void bar() { SB(); asm volatile("s_barrier" ::: "memory"); SB(); }
DI void bar_lds() { SB(); asm volatile("s_waitcnt lgkmcnt(0)\n\ts_barrier" ::: "memory"); SB(); }

DI void wait_flags(char* ws, unsigned off, u64 tag) {
    gu64* p = (gu64*)(ws + off);
    for (unsigned sp = 0; sp < (1u << 18); ++sp) {
        const u64 v = __hip_atomic_load(p, RLX_AGENT);
        if (__all(v == tag)) break;
    }
    asm volatile("" ::: "memory");
}
DI void set_flag(char* ws, unsigned off, u64 tag, unsigned lane) {
    vmwait<0>();
    if (lane == 0) __hip_atomic_store((gu64*)(ws + off), tag, RLX_AGENT);
}

#define STAMP(i)
#define STAMP_OUT()

struct Ctx {
    char* ws;
    rsrc_t rws, rx, rwqkv;
    u64 tag;
    unsigned b, w;
    DI unsigned g() const { return b & 7; }
    DI unsigned r() const { return b >> 3; }
    DI unsigned col8() const { return 8 * (b & 1) + ((b >> 3) & 7); }
    DI unsigned quad() const { return 16 * ((b & 7) >> 1) + 4 * (b >> 6); }
    DI unsigned fp() const { return 4 * (b & 1) + ((b >> 3) & 3); }
    DI unsigned pair() const { return 16 * ((b & 7) >> 1) + 2 * (b >> 5); }
};
DI Ctx fresh(const Ctx& c0) { Ctx c = c0; asm volatile("" : "+s"(c.b), "+s"(c.w)); return c; }

DI void finish_stats(float s, float q, float& mu, float& rs) {
    mu = s * (1.f / DM);
    rs = __builtin_amdgcn_rsqf(fmaxf(q * (1.f / DM) - mu * mu, 0.f) + 1e-5f);
}
DI void load_stats(const Ctx& c, unsigned off, unsigned tt, unsigned lane, u32x2 (&sp)[8]) {
    const unsigned vo = (lane & 31) * 8 + (lane >> 5) * 2048;
#pragma unroll
    for (int i = 0; i < 8; ++i) sp[i] = ld8<SC1>(c.rws, vo + i * 256, off + tt * 4096);
}
DI void reduce_stats(const u32x2 (&sp)[8], float& mu, float& rs) {
    float s = 0.f, q = 0.f;
#pragma unroll
    for (int i = 0; i < 8; ++i) { s += bc<float>(sp[i].x); q += bc<float>(sp[i].y); }
    finish_stats(swap32_add(s), swap32_add(q), mu, rs);
}

template <bool FIRST> DI unsigned qkv_w_voff(unsigned lane) {
    const unsigned row = lane >> 2, src = FIRST ? (row >> 2) * 8 + (row & 3) : row;
    return src * 3072 + (((lane & 3) ^ ((row >> 1) & 2)) * 16);
}
template <bool FIRST> DI void qkv_w_piece(const Ctx& c, unsigned vo, unsigned chunk, unsigned p, unsigned la) {
    const unsigned s = p >> 2, pc = p & 3, row0 = 64 * chunk + (FIRST ? 32 * (pc >> 1) + 4 * (pc & 1) : 16 * pc);
    dma<false, 0>(c.rwqkv, vo, row0 * 3072 + s * 1024 + c.col8() * 64, la);
}
DI unsigned qkv_w_lane(unsigned lane) { const unsigned q = lane >> 4; return (4 * q + ((lane & 15) >> 2)) * 64 + (q & 1) * 32 + (lane & 3) * 8; }

struct QkvAcc { f32x4 t[6][2]; };
template <int I> DI void qkv_mma(QkvAcc& a, u32x4 wf, u32x4 x0, u32x4 x1) {
    if constexpr (I < 4) { a.t[I][0] = mfma16(wf, x0, a.t[I][0]); PIN(a.t[I][0]); a.t[I][1] = mfma16(wf, x1, a.t[I][1]); PIN(a.t[I][1]); }
    else { a.t[I][0] = mfma16(x0, wf, a.t[I][0]); PIN(a.t[I][0]); a.t[I][1] = mfma16(x1, wf, a.t[I][1]); PIN(a.t[I][1]); }
}

DI void qkv_epilogue(const Ctx& c, unsigned layer, unsigned tt, unsigned lane, const QkvAcc& a) {
    const unsigned cc = lane & 15, q = lane >> 4, j = c.col8(), tl = c.w * 32;
    u32x2 p[6][2];
#pragma unroll
    for (int h = 0; h < 2; ++h) {
        const float mu = lds_ld<float>(L_MU + (tl + 16 * h + cc) * 4), rs = lds_ld<float>(L_RS + (tl + 16 * h + cc) * 4);
        const float rq = rs * (0.25f * 1.4426950408889634f);
        const f32x4 m4 = lds_ld<f32x4>(L_MU + (tl + 16 * h + 4 * q) * 4), r4 = lds_ld<f32x4>(L_RS + (tl + 16 * h + 4 * q) * 4);
#pragma unroll
        for (int i = 0; i < 4; ++i) {
            const f32x4 cs = lds_ld<f32x4>(L_CSQ + (16 * i + 4 * q) * 4) + lds_ld<f32x4>(L_CSQ + (96 + 16 * i + 4 * q) * 4);
            const f32x4 y = (a.t[i][h] - mu * cs) * (i < 2 ? rq : rs);
            p[i][h] = u32x2{pk2(y[0], y[1]), pk2(y[2], y[3])};
        }
#pragma unroll
        for (int i = 4; i < 6; ++i) {
            const float cs = lds_ld<float>(L_CSQ + (16 * i + cc) * 4) + lds_ld<float>(L_CSQ + (96 + 16 * i + cc) * 4);
            const f32x4 y = (a.t[i][h] - m4 * cs) * r4;
            p[i][h] = u32x2{pk2(y[0], y[1]), pk2(y[2], y[3])};
        }
    }
    const unsigned so = layer * 2 * MB + (2 * j) * 65536 + tt * 1024, t = lane & 31;
    const unsigned vk = (((t & 3) | ((t >> 4) << 2) | (((t >> 2) & 3) << 3)) + (lane & 32)) * 16;
#pragma unroll
    for (int s = 0; s < 2; ++s) {
        st16<SC1>(merge16(p[2 + s][0], p[2 + s][1]), c.rws, vk, OFF_K + so + s * 65536);
        st16<SC1>(u32x4{p[4 + s][0].x, p[4 + s][0].y, p[4 + s][1].x, p[4 + s][1].y}, c.rws, lane * 16, OFF_V + so + s * 65536);
        st16<SC1>(merge16(p[s][0], p[s][1]), c.rws, lane * 16, OFF_Q + so + s * 65536);
    }
    set_flag(c.ws, OFF_FQKV + layer * 8192 + j * 512 + tt * 8, c.tag, lane);
}

DI void phaseA_mfma(const Ctx& c0) {
    const Ctx c = fresh(c0); const unsigned lane = lane_id();
    constexpr int R = 4, P = 3;
    const unsigned w = c.w, cc = lane & 15, q = lane >> 4;
    unsigned wl[4], xa[4];
    wl[0] = 16384 + qkv_w_lane(lane); wl[1] = wl[0] ^ 32; wl[2] = wl[0] + 2 * L_SLOT; wl[3] = wl[1] + 2 * L_SLOT;
    xa[0] = (32 * w + cc) * 128 + ((q ^ (cc >> 1)) * 16); xa[1] = xa[0] ^ 64; xa[2] = xa[0] + 2 * L_SLOT; xa[3] = xa[1] + 2 * L_SLOT;
#pragma unroll
    for (int i = 0; i < 4; ++i) { PIN(wl[i]); PIN(xa[i]); }
    auto wread = [&](int n) AI { const int k = n / 6, i = n % 6, sl = (k >> 1) & 3; return lds_tr(wl[(i & 1) + (sl & 2)] + (sl & 1) * L_SLOT + (i >> 1) * 4096 + (k & 1) * 2048, 1024); };
    auto xread = [&](int k, int h) AI { const int sl = (k >> 1) & 3; return lds_ld<u32x4>(xa[(k & 1) + (sl & 2)] + (sl & 1) * L_SLOT + h * 2048); };
    unsigned one = ONES;
    PIN(one);
    const u32x4 ones = {one, one, one, one};
    QkvAcc a = {};
    f32x4 sx[2] = {}, sq[2] = {};
    u32x4 wf[R], xf[2][2];
    bar();
    xf[0][0] = xread(0, 0); xf[0][1] = xread(0, 1);
#pragma unroll
    for (int n = 0; n < P; ++n) wf[n] = wread(n);
    static_for<0, 96>([&](auto ic) AI {
        constexpr int n = decltype(ic)::v, k = n / 6, i = n % 6;
        if constexpr (n + P < 96) wf[(n + P) % R] = wread(n + P);
        if constexpr (i == 2 && k < 15) { xf[(k + 1) & 1][0] = xread(k + 1, 0); xf[(k + 1) & 1][1] = xread(k + 1, 1); }
        qkv_mma<i>(a, wf[n % R], xf[k & 1][0], xf[k & 1][1]);
        if constexpr (i < 2) { sx[i] = mfma16(ones, xf[k & 1][i], sx[i]); PIN(sx[i]); sq[i] = mfma16(xf[k & 1][i], xf[k & 1][i], sq[i]); PIN(sq[i]); }
        SB();
        if constexpr (n % 12 == 11 && n < 95) bar();
    });
    STAMP(15);
#pragma unroll
    for (int h = 0; h < 2; ++h) {
        const float d = (cc & 2) ? ((cc & 1) ? sq[h][3] : sq[h][2]) : ((cc & 1) ? sq[h][1] : sq[h][0]);
        float mu, rs;
        finish_stats(sx[h][0], d, mu, rs);
        if (q == (cc >> 2)) { lds_st<float>(L_MU + (32 * w + 16 * h + cc) * 4, mu); lds_st<float>(L_RS + (32 * w + 16 * h + cc) * 4, rs); }
    }
    bar_lds();
    qkv_epilogue(c, 0, c.quad() + w, lane, a);
    bar();
}

DI void tile_load(rsrc_t r, unsigned pitch, unsigned rowA, unsigned rowB, bool swap, unsigned col, unsigned la, unsigned lane) {
    const unsigned vo = (lane >> 3) * pitch + (lane & 7) * 16;
#pragma unroll
    for (int p = 0; p < 4; ++p) {
        const unsigned sp = swap ? ((p >> 1) | ((p & 1) << 1)) : p;
        dma<false, 0>(r, vo, (((sp & 2) ? rowB : rowA) + 8 * (sp & 1)) * pitch + col * 2, la + p * 1024);
    }
}
DI u32x4 tile_frag(unsigned la, int ct, unsigned lane) { return lds_tr(la + (4 * (lane >> 4) + ((lane & 15) >> 2)) * 128 + (lane & 3) * 8 + ct * 32, 2048); }

DI void phaseA_help(const Ctx& c0, const void* wo, const void* wg, const void* wu, const void* wd) {
    const Ctx c = fresh(c0); const unsigned lane = lane_id();
    const unsigned hw = c.w - 4, tok0 = 32 * (c.quad() + hw);
    const unsigned vx0 = (lane >> 3) * 1024 + (((lane & 7) ^ (lane >> 4)) * 16), vx1 = vx0 ^ 64, vw = qkv_w_voff<true>(lane);
    auto chunk = [&](int n) AI {
        const unsigned sl = (n & 3) * L_SLOT;
#pragma unroll
        for (int p = 0; p < 4; ++p) dma<false, 0>(c.rx, (p & 1) ? vx1 : vx0, (tok0 + 8 * p) * 1024 + n * 128, sl + hw * 4096 + p * 1024);
#pragma unroll
        for (int p = 0; p < 3; ++p) qkv_w_piece<true>(c, vw, n, 3 * hw + p, sl + 16384 + (3 * hw + p) * 1024);
    };
    chunk(0); chunk(1); chunk(2); chunk(3);
    unsigned one = ONES;
    PIN(one);
    const u32x4 ones = {one, one, one, one};
    unsigned wl[3];
#pragma unroll
    for (int i = 0; i < 3; ++i) { const unsigned ti = 3 * (hw & 1) + i; wl[i] = 16384 + (qkv_w_lane(lane) ^ ((ti & 1) * 32)) + (ti >> 1) * 4096 + (hw >> 1) * 2048; }
    f32x4 cs[3] = {};
    const unsigned gh = c.b * 4 + hw, stA = hw * L_SLOT + 28672, stB = L_EX + hw * 4096;
    static_for<0, 8>([&](auto ic) AI {
        constexpr int n = decltype(ic)::v;
        if constexpr (n == 0) vmwait<14>(); else if constexpr (n <= 5) vmwait<7>();
        else if constexpr (n == 6) { if (gh < 448) vmwait<8>(); else vmwait<4>(); }
        bar_lds();
        if constexpr (n >= 1 && n <= 4) chunk(n + 3);
        if constexpr (n == 5) {
            if (gh < 896) { const unsigned m = gh / 448, u = gh % 448; tile_load(mkrsrc(m ? wu : wg, DM * FF * 2), FF * 2, 32 * (u & 15), 32 * (u & 15) + 16, false, 64 * (u >> 4), stA, lane); }
            else { const unsigned u = gh - 896; tile_load(mkrsrc(wo, DM * DM * 2), DM * 2, 32 * (u & 15), 32 * (u & 15) + 16, true, 64 * (u >> 4), stA, lane); }
            if (gh < 448) {
                const unsigned sg = gh % 56, pr = sg / 7, e = sg % 7, fa = 14 * pr + (e < 3 ? 2 * e : e < 6 ? 2 * e + 1 : 6), fb = e < 6 ? fa + 1 : fa + 7;
                tile_load(mkrsrc(wd, FF * DM * 2), DM * 2, 16 * fa, 16 * fb, false, 64 * (gh / 56), stB, lane);
            }
        }
#pragma unroll
        for (int i = 0; i < 3; ++i) { cs[i] = mfma16(ones, lds_tr(wl[i] + (n & 3) * L_SLOT, 1024), cs[i]); PIN(cs[i]); }
    });
    if (lane < 16) {
#pragma unroll
        for (int i = 0; i < 3; ++i) lds_st<float>(L_CSQ + ((hw >> 1) * 96 + 48 * (hw & 1) + 16 * i + lane) * 4, cs[i][0]);
    }
    bar_lds();
    vmwait<0>();
    const unsigned vo = lane * 16;
    if (gh < 896) {
        const unsigned m = gh / 448, u = gh % 448, kb = u & 15, cb = u >> 4;
        float s[4];
#pragma unroll
        for (int ct = 0; ct < 4; ++ct) {
            const u32x4 f = tile_frag(stA, ct, lane);
            const unsigned fb = 4 * cb + ct;
            st16<SC1>(f, c.rws, vo, OFF_WG + ((((fb / 7) * 16 + kb) * 7 + fb % 7) * 2 + m) * 1024);
            s[ct] = mfma16(ones, f, f32x4{})[0];
        }
        const float sv = (lane & 32) ? ((lane & 16) ? s[3] : s[2]) : ((lane & 16) ? s[1] : s[0]);
        __builtin_amdgcn_raw_buffer_store_b32(bc<unsigned>(sv), c.rws, lane * 4, OFF_CSG + (kb * 2 + m) * 7168 + cb * 256, SC1);
    } else {
        const unsigned u = gh - 896, kb = u & 15, cb = u >> 4;
#pragma unroll
        for (int ct = 0; ct < 4; ++ct) stfrag(tile_frag(stA, ct, lane), c.rws, vo, OFF_WO + ((cb * 8 + (kb & 7)) * 2 + (kb >> 3)) * 4096, ct);
    }
    if (gh < 448) {
        const unsigned sg = gh % 56;
#pragma unroll
        for (int ct = 0; ct < 4; ++ct) stfrag(tile_frag(stB, ct, lane), c.rws, vo, OFF_WD + (((gh / 56) * 28 + sg % 28) * 2 + sg / 28) * 4096, ct);
    }
    vmwait<0>();
    bar_lds();
    if (hw == 3 && lane == 0) __hip_atomic_store((gu64*)(c.ws + OFF_FW + c.b * 8), lds_ld<u64>(L_VOTE + 8), RLX_AGENT);
}

DI void phaseF(const Ctx& c0) {
    const Ctx c = fresh(c0); const unsigned lane = lane_id();
    constexpr int R = 5, P = 4;
    const unsigned w = c.w, tt = c.quad() + w, vo = lane * 16;
    wait_flags(c.ws, OFF_FX2 + tt * 128 + (lane & 15) * 8, c.tag);
    STAMP(27);
    u32x4 tok[32], wf[R];
    u32x2 sp[8];
#pragma unroll
    for (int s = 0; s < 8; ++s) tok[s] = ldfrag(c.rws, vo, OFF_X2 + tt * 32768, s);
    load_stats(c, OFF_ST2, tt, lane, sp);
#pragma unroll
    for (int s = 8; s < 32; ++s) tok[s] = ldfrag(c.rws, vo, OFF_X2 + tt * 32768, s);
    bar();
    unsigned wl[4];
    wl[0] = L_SLOT + qkv_w_lane(lane); wl[1] = wl[0] ^ 32; wl[2] = wl[0] + 49152; wl[3] = wl[1] + 49152;
#pragma unroll
    for (int i = 0; i < 4; ++i) PIN(wl[i]);
    auto wread = [&](int n) AI { const int k = n / 6, i = n % 6; return lds_tr(wl[(i & 1) + ((k >> 3) << 1)] + ((k >> 1) & 3) * 12288 + (i >> 1) * 4096 + (k & 1) * 2048, 1024); };
#pragma unroll
    for (int n = 0; n < P; ++n) wf[n] = wread(n);
    QkvAcc a = {};
    static_for<0, 96>([&](auto ic) AI {
        constexpr int n = decltype(ic)::v, k = n / 6, i = n % 6;
        if constexpr (n + P < 96) wf[(n + P) % R] = wread(n + P);
        qkv_mma<i>(a, wf[n % R], tok[2 * k], tok[2 * k + 1]);
        if constexpr (n == 30) {
            float mu, rs;
            reduce_stats(sp, mu, rs);
            if (lane < 32) { lds_st<float>(L_MU + (32 * w + lane) * 4, mu); lds_st<float>(L_RS + (32 * w + lane) * 4, rs); }
        }
        SB();
    });
    STAMP(28);
    bar_lds();
    qkv_epilogue(c, 1, tt, lane, a);
}

template <int N> DI void issue_chunk(const Ctx& c, unsigned lane) {
    constexpr int np = (N >= 2 && N <= 9) ? 7 : 8;
    const unsigned hw = c.w - 4;
    const unsigned src = (N < 2 ? OFF_WO + c.fp() * 65536 + N * 32768 : N < 10 ? OFF_WG + c.col8() * 229376 + (N - 2) * 28672 : OFF_WD + c.fp() * 229376 + (N - 10) * 32768) + hw * (np * 1024);
    const unsigned dst = (N & 3) * L_SLOT + hw * (np * 1024), vo = lane * 16;
    dma<true, 0>(c.rws, vo, src, dst); dma<true, 1024>(c.rws, vo, src, dst); dma<true, 2048>(c.rws, vo, src, dst); dma<true, 3072>(c.rws, vo, src, dst);
    dma<true, 0>(c.rws, vo, src + 4096, dst + 4096); dma<true, 1024>(c.rws, vo, src + 4096, dst + 4096); dma<true, 2048>(c.rws, vo, src + 4096, dst + 4096);
    if constexpr (np == 8) dma<true, 3072>(c.rws, vo, src + 4096, dst + 4096);
}

#define ATT_FILL \
    "v_mov_b32 v160, %[cv]\n\tv_mov_b32 v161, %[cv]\n\tv_mov_b32 v162, %[cv]\n\tv_mov_b32 v163, %[cv]\n\tv_mov_b32 v164, %[cv]\n\tv_mov_b32 v165, %[cv]\n\tv_mov_b32 v166, %[cv]\n\t" \
    "v_mov_b32 v167, %[cv]\n\tv_mov_b32 v168, %[cv]\n\tv_mov_b32 v169, %[cv]\n\tv_mov_b32 v170, %[cv]\n\tv_mov_b32 v171, %[cv]\n\tv_mov_b32 v172, %[cv]\n\tv_mov_b32 v173, %[cv]\n\t" \
    "v_mov_b32 v174, %[cv]\n\tv_mov_b32 v175, %[cv]\n\tv_mov_b32 v176, %[cv]\n\tv_mov_b32 v177, %[cv]\n\tv_mov_b32 v178, %[cv]\n\tv_mov_b32 v179, %[cv]\n\tv_mov_b32 v180, %[cv]\n\t" \
    "v_mov_b32 v181, %[cv]\n\tv_mov_b32 v182, %[cv]\n\tv_mov_b32 v183, %[cv]\n\tv_mov_b32 v184, %[cv]\n\tv_mov_b32 v185, %[cv]\n\tv_mov_b32 v186, %[cv]\n\tv_mov_b32 v187, %[cv]\n\t" \
    "v_mov_b32 v188, %[cv]\n\tv_mov_b32 v189, %[cv]\n\tv_mov_b32 v190, %[cv]\n\tv_mov_b32 v191, %[cv]\n\tv_mov_b32 v192, %[cv]\n\tv_mov_b32 v193, %[cv]\n\tv_mov_b32 v194, %[cv]\n\t" \
    "v_mov_b32 v195, %[cv]\n\tv_mov_b32 v196, %[cv]\n\tv_mov_b32 v197, %[cv]\n\tv_mov_b32 v198, %[cv]\n\tv_mov_b32 v199, %[cv]\n\tv_mov_b32 v200, %[cv]\n\tv_mov_b32 v201, %[cv]\n\t" \
    "v_mov_b32 v202, %[cv]\n\tv_mov_b32 v203, %[cv]\n\tv_mov_b32 v204, %[cv]\n\tv_mov_b32 v205, %[cv]\n\tv_mov_b32 v206, %[cv]\n\tv_mov_b32 v207, %[cv]\n\tv_mov_b32 v208, %[cv]\n\t" \
    "v_mov_b32 v209, %[cv]\n\tv_mov_b32 v210, %[cv]\n\tv_mov_b32 v211, %[cv]\n\tv_mov_b32 v212, %[cv]\n\tv_mov_b32 v213, %[cv]\n\tv_mov_b32 v214, %[cv]\n\tv_mov_b32 v215, %[cv]\n\t" \
    "v_mov_b32 v216, %[cv]\n\tv_mov_b32 v217, %[cv]\n\tv_mov_b32 v218, %[cv]\n\tv_mov_b32 v219, %[cv]\n\tv_mov_b32 v220, %[cv]\n\tv_mov_b32 v221, %[cv]\n\tv_mov_b32 v222, %[cv]\n\t" \
    "v_mov_b32 v223, %[cv]\n\tv_mov_b32 v48, 0\n\tv_mov_b32 v49, 0\n\tv_mov_b32 v50, 0\n\tv_mov_b32 v51, 0\n\tv_mov_b32 v52, 0\n\tv_mov_b32 v53, 0\n\tv_mov_b32 v54, 0\n\tv_mov_b32 v55, 0\n\t" \
    "v_mov_b32 v56, 0\n\tv_mov_b32 v57, 0\n\tv_mov_b32 v58, 0\n\tv_mov_b32 v59, 0\n\tv_mov_b32 v60, 0\n\tv_mov_b32 v61, 0\n\tv_mov_b32 v62, 0\n\tv_mov_b32 v63, 0\n\tv_mov_b32 v64, 0\n\t" \
    "v_mov_b32 v65, 0\n\tv_mov_b32 v66, 0\n\tv_mov_b32 v67, 0\n\tv_mov_b32 v68, 0\n\tv_mov_b32 v69, 0\n\tv_mov_b32 v70, 0\n\tv_mov_b32 v71, 0\n\tv_mov_b32 v72, 0\n\tv_mov_b32 v73, 0\n\t" \
    "v_mov_b32 v74, 0\n\tv_mov_b32 v75, 0\n\tv_mov_b32 v76, 0\n\tv_mov_b32 v77, 0\n\tv_mov_b32 v78, 0\n\tv_mov_b32 v79, 0\n\t"
#define ATT_FIRST \
    "s_nop 4\n\ts_mov_b32 m0, %[qd]\n\ts_nop 0\n\tbuffer_load_dwordx4 v95, %[rs], %[sq] offen sc1 lds\n\tbuffer_load_dwordx4 v[80:83], v95, %[rs], %[sq] offen sc1\n\t" \
    "buffer_load_dwordx4 v[128:131], v95, %[rs], %[sk] offen offset:0 sc1\n\ts_mov_b64 exec, %[vm]\n\tbuffer_load_dwordx4 v[160:163], v96, %[rs], %[sv] offen offset:0 sc1\n\t" \
    "buffer_load_dwordx4 v[164:167], v96, %[rs], %[sv] offen offset:512 sc1\n\ts_mov_b64 exec, -1\n\tbuffer_load_dwordx4 v[132:135], v95, %[rs], %[sk] offen offset:1024 sc1\n\t" \
    "s_mov_b64 exec, %[vm]\n\tbuffer_load_dwordx4 v[168:171], v96, %[rs], %[sv] offen offset:1024 sc1\n\tbuffer_load_dwordx4 v[172:175], v96, %[rs], %[sv] offen offset:1536 sc1\n\t" \
    "s_mov_b64 exec, -1\n\tbuffer_load_dwordx4 v[136:139], v95, %[rs], %[sk] offen offset:2048 sc1\n\ts_mov_b64 exec, %[vm]\n\t" \
    "buffer_load_dwordx4 v[176:179], v96, %[rs], %[sv] offen offset:2048 sc1\n\tbuffer_load_dwordx4 v[180:183], v96, %[rs], %[sv] offen offset:2560 sc1\n\ts_mov_b64 exec, -1\n\t" \
    "buffer_load_dwordx4 v[140:143], v95, %[rs], %[sk] offen offset:3072 sc1\n\ts_mov_b64 exec, %[vm]\n\tbuffer_load_dwordx4 v[184:187], v96, %[rs], %[sv] offen offset:3072 sc1\n\t" \
    "buffer_load_dwordx4 v[188:191], v96, %[rs], %[sv] offen offset:3584 sc1\n\ts_mov_b64 exec, -1\n\ts_add_u32 %[sk], %[sk], 0x1000\n\ts_add_u32 %[sv], %[sv], 0x1000\n\t" \
    "buffer_load_dwordx4 v[144:147], v95, %[rs], %[sk] offen offset:0 sc1\n\ts_mov_b64 exec, %[vm]\n\tbuffer_load_dwordx4 v[192:195], v96, %[rs], %[sv] offen offset:0 sc1\n\t" \
    "buffer_load_dwordx4 v[196:199], v96, %[rs], %[sv] offen offset:512 sc1\n\ts_mov_b64 exec, -1\n\tbuffer_load_dwordx4 v[148:151], v95, %[rs], %[sk] offen offset:1024 sc1\n\t" \
    "s_mov_b64 exec, %[vm]\n\tbuffer_load_dwordx4 v[200:203], v96, %[rs], %[sv] offen offset:1024 sc1\n\tbuffer_load_dwordx4 v[204:207], v96, %[rs], %[sv] offen offset:1536 sc1\n\t" \
    "s_mov_b64 exec, -1\n\tbuffer_load_dwordx4 v[152:155], v95, %[rs], %[sk] offen offset:2048 sc1\n\ts_mov_b64 exec, %[vm]\n\t" \
    "buffer_load_dwordx4 v[208:211], v96, %[rs], %[sv] offen offset:2048 sc1\n\tbuffer_load_dwordx4 v[212:215], v96, %[rs], %[sv] offen offset:2560 sc1\n\ts_mov_b64 exec, -1\n\t" \
    "buffer_load_dwordx4 v[156:159], v95, %[rs], %[sk] offen offset:3072 sc1\n\ts_mov_b64 exec, %[vm]\n\tbuffer_load_dwordx4 v[216:219], v96, %[rs], %[sv] offen offset:3072 sc1\n\t" \
    "buffer_load_dwordx4 v[220:223], v96, %[rs], %[sv] offen offset:3584 sc1\n\ts_mov_b64 exec, -1\n\ts_waitcnt vmcnt(23)\n\tv_mfma_f32_32x32x16_bf16 v[16:31], v[128:131], v[80:83], 0\n\t" \
    "s_nop 15\n\tv_exp_f32 v16, v16\n\tv_exp_f32 v17, v17\n\tv_exp_f32 v18, v18\n\tv_exp_f32 v19, v19\n\tv_exp_f32 v20, v20\n\ts_waitcnt vmcnt(20)\n\t" \
    "v_mfma_f32_32x32x16_bf16 v[32:47], v[132:135], v[80:83], 0\n\tv_exp_f32 v21, v21\n\tv_exp_f32 v22, v22\n\tv_exp_f32 v23, v23\n\tv_exp_f32 v24, v24\n\tv_exp_f32 v25, v25\n\t" \
    "v_exp_f32 v26, v26\n\tv_smfmac_f32_32x32x32_bf16 v[48:63], v[160:163], v[16:23], v94\n\tv_exp_f32 v27, v27\n\tv_exp_f32 v28, v28\n\tv_exp_f32 v29, v29\n\tv_exp_f32 v30, v30\n\t" \
    "v_exp_f32 v31, v31\n\tv_exp_f32 v32, v32\n\tv_exp_f32 v33, v33\n\tv_smfmac_f32_32x32x32_bf16 v[48:63], v[164:167], v[24:31], v94\n\tds_write_b128 v88, v[64:67] offset:18432\n\t" \
    "ds_write_b128 v88, v[68:71] offset:19456\n\tds_write_b32 v89, v72 offset:20480\n\tv_exp_f32 v34, v34\n\tv_exp_f32 v35, v35\n\tv_exp_f32 v36, v36\n\ts_waitcnt vmcnt(17)\n\t" \
    "v_mfma_f32_32x32x16_bf16 v[16:31], v[136:139], v[80:83], 0\n\tv_exp_f32 v37, v37\n\tv_exp_f32 v38, v38\n\tv_exp_f32 v39, v39\n\tv_exp_f32 v40, v40\n\tv_exp_f32 v41, v41\n\t" \
    "v_exp_f32 v42, v42\n\tv_smfmac_f32_32x32x32_bf16 v[48:63], v[168:171], v[32:39], v94\n\tv_exp_f32 v43, v43\n\tv_exp_f32 v44, v44\n\tv_exp_f32 v45, v45\n\tv_exp_f32 v46, v46\n\t" \
    "v_exp_f32 v47, v47\n\tv_exp_f32 v16, v16\n\tv_exp_f32 v17, v17\n\tv_smfmac_f32_32x32x32_bf16 v[48:63], v[172:175], v[40:47], v94\n\tv_exp_f32 v18, v18\n\tv_exp_f32 v19, v19\n\t" \
    "v_exp_f32 v20, v20\n\ts_waitcnt vmcnt(14)\n\tv_mfma_f32_32x32x16_bf16 v[32:47], v[140:143], v[80:83], 0\n\tv_exp_f32 v21, v21\n\tv_exp_f32 v22, v22\n\tv_exp_f32 v23, v23\n\t" \
    "v_exp_f32 v24, v24\n\tv_exp_f32 v25, v25\n\tv_exp_f32 v26, v26\n\tv_smfmac_f32_32x32x32_bf16 v[48:63], v[176:179], v[16:23], v94\n\tv_exp_f32 v27, v27\n\tv_exp_f32 v28, v28\n\t" \
    "v_exp_f32 v29, v29\n\tv_exp_f32 v30, v30\n\tv_exp_f32 v31, v31\n\tv_exp_f32 v32, v32\n\tv_exp_f32 v33, v33\n\tv_smfmac_f32_32x32x32_bf16 v[48:63], v[180:183], v[24:31], v94\n\t" \
    "v_exp_f32 v34, v34\n\tv_exp_f32 v35, v35\n\tv_exp_f32 v36, v36\n\ts_waitcnt vmcnt(11)\n\tv_mfma_f32_32x32x16_bf16 v[16:31], v[144:147], v[80:83], 0\n\tv_exp_f32 v37, v37\n\t" \
    "v_exp_f32 v38, v38\n\tv_exp_f32 v39, v39\n\tv_exp_f32 v40, v40\n\tv_exp_f32 v41, v41\n\tv_exp_f32 v42, v42\n\tv_smfmac_f32_32x32x32_bf16 v[48:63], v[184:187], v[32:39], v94\n\t" \
    "v_exp_f32 v43, v43\n\tv_exp_f32 v44, v44\n\tv_exp_f32 v45, v45\n\tv_exp_f32 v46, v46\n\tv_exp_f32 v47, v47\n\tv_exp_f32 v16, v16\n\tv_exp_f32 v17, v17\n\t" \
    "v_smfmac_f32_32x32x32_bf16 v[48:63], v[188:191], v[40:47], v94\n\ts_waitcnt lgkmcnt(0)\n\ts_barrier\n\tds_read_b128 v[64:67], v90 offset:18432\n\tds_read_b128 v[68:71], v90 offset:19456\n\t" \
    "ds_read_b32 v72, v91 offset:20480\n\ts_add_u32 %[qo], %[qo], 0x400\n\ts_and_b32 %[qo], %[qo], 0x1fff\n\tv_add_u32 v97, %[qo], v98\n\tds_read_b128 v[84:87], v97\n\tv_exp_f32 v18, v18\n\t" \
    "v_exp_f32 v19, v19\n\tv_exp_f32 v20, v20\n\ts_waitcnt vmcnt(8)\n\tv_mfma_f32_32x32x16_bf16 v[32:47], v[148:151], v[80:83], 0\n\tv_exp_f32 v21, v21\n\tv_exp_f32 v22, v22\n\t" \
    "v_exp_f32 v23, v23\n\tv_exp_f32 v24, v24\n\tv_exp_f32 v25, v25\n\tv_exp_f32 v26, v26\n\tv_smfmac_f32_32x32x32_bf16 v[48:63], v[192:195], v[16:23], v94\n\tv_exp_f32 v27, v27\n\t" \
    "v_exp_f32 v28, v28\n\tv_exp_f32 v29, v29\n\tv_exp_f32 v30, v30\n\tv_exp_f32 v31, v31\n\tv_exp_f32 v32, v32\n\tv_exp_f32 v33, v33\n\t" \
    "v_smfmac_f32_32x32x32_bf16 v[48:63], v[196:199], v[24:31], v94\n\tv_exp_f32 v34, v34\n\tv_exp_f32 v35, v35\n\tv_exp_f32 v36, v36\n\ts_waitcnt vmcnt(5)\n\t" \
    "v_mfma_f32_32x32x16_bf16 v[16:31], v[152:155], v[80:83], 0\n\tv_exp_f32 v37, v37\n\tv_exp_f32 v38, v38\n\tv_exp_f32 v39, v39\n\tv_exp_f32 v40, v40\n\tv_exp_f32 v41, v41\n\t" \
    "v_exp_f32 v42, v42\n\tv_smfmac_f32_32x32x32_bf16 v[48:63], v[200:203], v[32:39], v94\n\tv_exp_f32 v43, v43\n\tv_exp_f32 v44, v44\n\tv_exp_f32 v45, v45\n\tv_exp_f32 v46, v46\n\t" \
    "v_exp_f32 v47, v47\n\tv_exp_f32 v16, v16\n\tv_exp_f32 v17, v17\n\tv_smfmac_f32_32x32x32_bf16 v[48:63], v[204:207], v[40:47], v94\n\tv_exp_f32 v18, v18\n\tv_exp_f32 v19, v19\n\t" \
    "v_exp_f32 v20, v20\n\ts_waitcnt vmcnt(2)\n\tv_mfma_f32_32x32x16_bf16 v[32:47], v[156:159], v[80:83], 0\n\tv_exp_f32 v21, v21\n\tv_exp_f32 v22, v22\n\tv_exp_f32 v23, v23\n\t" \
    "v_exp_f32 v24, v24\n\tv_exp_f32 v25, v25\n\tv_exp_f32 v26, v26\n\tv_smfmac_f32_32x32x32_bf16 v[48:63], v[208:211], v[16:23], v94\n\tv_exp_f32 v27, v27\n\tv_exp_f32 v28, v28\n\t" \
    "v_exp_f32 v29, v29\n\tv_exp_f32 v30, v30\n\tv_exp_f32 v31, v31\n\tv_exp_f32 v32, v32\n\tv_exp_f32 v33, v33\n\tv_smfmac_f32_32x32x32_bf16 v[48:63], v[212:215], v[24:31], v94\n\t" \
    "v_exp_f32 v34, v34\n\tv_exp_f32 v35, v35\n\tv_exp_f32 v36, v36\n\ts_waitcnt vmcnt(0)\n\ts_waitcnt lgkmcnt(0)\n\tv_mfma_f32_32x32x16_bf16 v[16:31], v[128:131], v[84:87], 0\n\t" \
    "v_exp_f32 v37, v37\n\tv_exp_f32 v38, v38\n\tv_exp_f32 v39, v39\n\tv_exp_f32 v40, v40\n\tv_exp_f32 v41, v41\n\tv_exp_f32 v42, v42\n\t" \
    "v_smfmac_f32_32x32x32_bf16 v[48:63], v[216:219], v[32:39], v94\n\tv_exp_f32 v43, v43\n\tv_exp_f32 v44, v44\n\tv_exp_f32 v45, v45\n\tv_exp_f32 v46, v46\n\tv_exp_f32 v47, v47\n\t" \
    "v_exp_f32 v16, v16\n\tv_exp_f32 v17, v17\n\tv_smfmac_f32_32x32x32_bf16 v[48:63], v[220:223], v[40:47], v94\n\tv_exp_f32 v18, v18\n\tv_exp_f32 v19, v19\n\tv_exp_f32 v20, v20\n\t" \
    "v_mfma_f32_32x32x16_bf16 v[32:47], v[132:135], v[84:87], 0\n\tv_exp_f32 v21, v21\n\tv_exp_f32 v22, v22\n\tv_exp_f32 v23, v23\n\tv_exp_f32 v24, v24\n\tv_exp_f32 v25, v25\n\t" \
    "v_exp_f32 v26, v26\n\tv_smfmac_f32_32x32x32_bf16 v[64:79], v[160:163], v[16:23], v94\n\tv_exp_f32 v27, v27\n\tv_exp_f32 v28, v28\n\tv_exp_f32 v29, v29\n\tv_exp_f32 v30, v30\n\t" \
    "v_exp_f32 v31, v31\n\tv_exp_f32 v32, v32\n\tv_exp_f32 v33, v33\n\tv_smfmac_f32_32x32x32_bf16 v[64:79], v[164:167], v[24:31], v94\n\tds_write_b128 v88, v[48:51] offset:0\n\t" \
    "ds_write_b128 v88, v[52:55] offset:1024\n\tds_write_b32 v89, v56 offset:2048\n\tv_exp_f32 v34, v34\n\tv_exp_f32 v35, v35\n\tv_exp_f32 v36, v36\n\t" \
    "v_mfma_f32_32x32x16_bf16 v[16:31], v[136:139], v[84:87], 0\n\tv_exp_f32 v37, v37\n\tv_exp_f32 v38, v38\n\tv_exp_f32 v39, v39\n\tv_exp_f32 v40, v40\n\tv_exp_f32 v41, v41\n\t" \
    "v_exp_f32 v42, v42\n\tv_smfmac_f32_32x32x32_bf16 v[64:79], v[168:171], v[32:39], v94\n\tv_exp_f32 v43, v43\n\tv_exp_f32 v44, v44\n\tv_exp_f32 v45, v45\n\tv_exp_f32 v46, v46\n\t" \
    "v_exp_f32 v47, v47\n\tv_exp_f32 v16, v16\n\tv_exp_f32 v17, v17\n\tv_smfmac_f32_32x32x32_bf16 v[64:79], v[172:175], v[40:47], v94\n\tv_exp_f32 v18, v18\n\tv_exp_f32 v19, v19\n\t" \
    "v_exp_f32 v20, v20\n\tv_mfma_f32_32x32x16_bf16 v[32:47], v[140:143], v[84:87], 0\n\tv_exp_f32 v21, v21\n\tv_exp_f32 v22, v22\n\tv_exp_f32 v23, v23\n\tv_exp_f32 v24, v24\n\t" \
    "v_exp_f32 v25, v25\n\tv_exp_f32 v26, v26\n\tv_smfmac_f32_32x32x32_bf16 v[64:79], v[176:179], v[16:23], v94\n\tv_exp_f32 v27, v27\n\tv_exp_f32 v28, v28\n\tv_exp_f32 v29, v29\n\t" \
    "v_exp_f32 v30, v30\n\tv_exp_f32 v31, v31\n\tv_exp_f32 v32, v32\n\tv_exp_f32 v33, v33\n\tv_smfmac_f32_32x32x32_bf16 v[64:79], v[180:183], v[24:31], v94\n\tv_exp_f32 v34, v34\n\t" \
    "v_exp_f32 v35, v35\n\tv_exp_f32 v36, v36\n\tv_mfma_f32_32x32x16_bf16 v[16:31], v[144:147], v[84:87], 0\n\tv_exp_f32 v37, v37\n\tv_exp_f32 v38, v38\n\tv_exp_f32 v39, v39\n\t" \
    "v_exp_f32 v40, v40\n\tv_exp_f32 v41, v41\n\tv_exp_f32 v42, v42\n\tv_smfmac_f32_32x32x32_bf16 v[64:79], v[184:187], v[32:39], v94\n\tv_exp_f32 v43, v43\n\tv_exp_f32 v44, v44\n\t" \
    "v_exp_f32 v45, v45\n\tv_exp_f32 v46, v46\n\tv_exp_f32 v47, v47\n\tv_exp_f32 v16, v16\n\tv_exp_f32 v17, v17\n\tv_smfmac_f32_32x32x32_bf16 v[64:79], v[188:191], v[40:47], v94\n\t" \
    "s_waitcnt lgkmcnt(0)\n\ts_barrier\n\tds_read_b128 v[48:51], v92 offset:0\n\tds_read_b128 v[52:55], v92 offset:1024\n\tds_read_b32 v56, v93 offset:2048\n\ts_add_u32 %[qo], %[qo], 0x400\n\t" \
    "s_and_b32 %[qo], %[qo], 0x1fff\n\tv_add_u32 v97, %[qo], v98\n\tds_read_b128 v[80:83], v97\n\tbuffer_load_dwordx2 v[100:101], v108, %[rs], %[sfw] offen offset:0 sc1\n\t" \
    "buffer_load_dwordx2 v[102:103], v108, %[rs], %[sfw] offen offset:512 sc1\n\tbuffer_load_dwordx2 v[104:105], v108, %[rs], %[sfw] offen offset:1024 sc1\n\t" \
    "buffer_load_dwordx2 v[106:107], v108, %[rs], %[sfw] offen offset:1536 sc1\n\tv_exp_f32 v18, v18\n\tv_exp_f32 v19, v19\n\tv_exp_f32 v20, v20\n\t" \
    "v_mfma_f32_32x32x16_bf16 v[32:47], v[148:151], v[84:87], 0\n\tv_exp_f32 v21, v21\n\tv_exp_f32 v22, v22\n\tv_exp_f32 v23, v23\n\tv_exp_f32 v24, v24\n\tv_exp_f32 v25, v25\n\t" \
    "v_exp_f32 v26, v26\n\tv_smfmac_f32_32x32x32_bf16 v[64:79], v[192:195], v[16:23], v94\n\tv_exp_f32 v27, v27\n\tv_exp_f32 v28, v28\n\tv_exp_f32 v29, v29\n\tv_exp_f32 v30, v30\n\t" \
    "v_exp_f32 v31, v31\n\tv_exp_f32 v32, v32\n\tv_exp_f32 v33, v33\n\tv_smfmac_f32_32x32x32_bf16 v[64:79], v[196:199], v[24:31], v94\n\tv_exp_f32 v34, v34\n\tv_exp_f32 v35, v35\n\t" \
    "v_exp_f32 v36, v36\n\tv_mfma_f32_32x32x16_bf16 v[16:31], v[152:155], v[84:87], 0\n\tv_exp_f32 v37, v37\n\tv_exp_f32 v38, v38\n\tv_exp_f32 v39, v39\n\tv_exp_f32 v40, v40\n\t" \
    "v_exp_f32 v41, v41\n\tv_exp_f32 v42, v42\n\tv_smfmac_f32_32x32x32_bf16 v[64:79], v[200:203], v[32:39], v94\n\tv_exp_f32 v43, v43\n\tv_exp_f32 v44, v44\n\tv_exp_f32 v45, v45\n\t" \
    "v_exp_f32 v46, v46\n\tv_exp_f32 v47, v47\n\tv_exp_f32 v16, v16\n\tv_exp_f32 v17, v17\n\tv_smfmac_f32_32x32x32_bf16 v[64:79], v[204:207], v[40:47], v94\n\tv_exp_f32 v18, v18\n\t" \
    "v_exp_f32 v19, v19\n\tv_exp_f32 v20, v20\n\tv_mfma_f32_32x32x16_bf16 v[32:47], v[156:159], v[84:87], 0\n\tv_exp_f32 v21, v21\n\tv_exp_f32 v22, v22\n\tv_exp_f32 v23, v23\n\t" \
    "v_exp_f32 v24, v24\n\tv_exp_f32 v25, v25\n\tv_exp_f32 v26, v26\n\tv_smfmac_f32_32x32x32_bf16 v[64:79], v[208:211], v[16:23], v94\n\tv_exp_f32 v27, v27\n\tv_exp_f32 v28, v28\n\t" \
    "v_exp_f32 v29, v29\n\tv_exp_f32 v30, v30\n\tv_exp_f32 v31, v31\n\tv_exp_f32 v32, v32\n\tv_exp_f32 v33, v33\n\tv_smfmac_f32_32x32x32_bf16 v[64:79], v[212:215], v[24:31], v94\n\t" \
    "v_exp_f32 v34, v34\n\tv_exp_f32 v35, v35\n\tv_exp_f32 v36, v36\n\ts_waitcnt lgkmcnt(0)\n\tv_mfma_f32_32x32x16_bf16 v[16:31], v[128:131], v[80:83], 0\n\tv_exp_f32 v37, v37\n\t" \
    "v_exp_f32 v38, v38\n\tv_exp_f32 v39, v39\n\tv_exp_f32 v40, v40\n\tv_exp_f32 v41, v41\n\tv_exp_f32 v42, v42\n\tv_smfmac_f32_32x32x32_bf16 v[64:79], v[216:219], v[32:39], v94\n\t" \
    "v_exp_f32 v43, v43\n\tv_exp_f32 v44, v44\n\tv_exp_f32 v45, v45\n\tv_exp_f32 v46, v46\n\tv_exp_f32 v47, v47\n\tv_exp_f32 v16, v16\n\tv_exp_f32 v17, v17\n\t" \
    "v_smfmac_f32_32x32x32_bf16 v[64:79], v[220:223], v[40:47], v94\n\ts_waitcnt vmcnt(0)\n\ts_nop 7\n\t"
#define ATT_LOOP \
    "s_nop 4\n\tv_exp_f32 v18, v18\n\tv_exp_f32 v19, v19\n\tv_exp_f32 v20, v20\n\tv_mfma_f32_32x32x16_bf16 v[32:47], v[132:135], v[80:83], 0\n\tv_exp_f32 v21, v21\n\tv_exp_f32 v22, v22\n\t" \
    "v_exp_f32 v23, v23\n\tv_exp_f32 v24, v24\n\tv_exp_f32 v25, v25\n\tv_exp_f32 v26, v26\n\tv_smfmac_f32_32x32x32_bf16 v[48:63], v[160:163], v[16:23], v94\n\tv_exp_f32 v27, v27\n\t" \
    "v_exp_f32 v28, v28\n\tv_exp_f32 v29, v29\n\tv_exp_f32 v30, v30\n\tv_exp_f32 v31, v31\n\tv_exp_f32 v32, v32\n\tv_exp_f32 v33, v33\n\t" \
    "v_smfmac_f32_32x32x32_bf16 v[48:63], v[164:167], v[24:31], v94\n\tds_write_b128 v88, v[64:67] offset:18432\n\tds_write_b128 v88, v[68:71] offset:19456\n\t" \
    "ds_write_b32 v89, v72 offset:20480\n\tv_exp_f32 v34, v34\n\tv_exp_f32 v35, v35\n\tv_exp_f32 v36, v36\n\tv_mfma_f32_32x32x16_bf16 v[16:31], v[136:139], v[80:83], 0\n\tv_exp_f32 v37, v37\n\t" \
    "v_exp_f32 v38, v38\n\tv_exp_f32 v39, v39\n\tv_exp_f32 v40, v40\n\tv_exp_f32 v41, v41\n\tv_exp_f32 v42, v42\n\tv_smfmac_f32_32x32x32_bf16 v[48:63], v[168:171], v[32:39], v94\n\t" \
    "v_exp_f32 v43, v43\n\tv_exp_f32 v44, v44\n\tv_exp_f32 v45, v45\n\tv_exp_f32 v46, v46\n\tv_exp_f32 v47, v47\n\tv_exp_f32 v16, v16\n\tv_exp_f32 v17, v17\n\ts_mov_b32 m0, %[pd]\n\ts_nop 0\n\t" \
    "buffer_load_dwordx4 v95, %[rs], %[ps] offen offset:0 sc1 lds\n\tv_smfmac_f32_32x32x32_bf16 v[48:63], v[172:175], v[40:47], v94\n\tv_exp_f32 v18, v18\n\tv_exp_f32 v19, v19\n\t" \
    "v_exp_f32 v20, v20\n\tv_mfma_f32_32x32x16_bf16 v[32:47], v[140:143], v[80:83], 0\n\tv_exp_f32 v21, v21\n\tv_exp_f32 v22, v22\n\tv_exp_f32 v23, v23\n\tv_exp_f32 v24, v24\n\t" \
    "v_exp_f32 v25, v25\n\tv_exp_f32 v26, v26\n\tv_smfmac_f32_32x32x32_bf16 v[48:63], v[176:179], v[16:23], v94\n\tv_exp_f32 v27, v27\n\tv_exp_f32 v28, v28\n\tv_exp_f32 v29, v29\n\t" \
    "v_exp_f32 v30, v30\n\tv_exp_f32 v31, v31\n\tv_exp_f32 v32, v32\n\tv_exp_f32 v33, v33\n\tv_smfmac_f32_32x32x32_bf16 v[48:63], v[180:183], v[24:31], v94\n\tv_exp_f32 v34, v34\n\t" \
    "v_exp_f32 v35, v35\n\tv_exp_f32 v36, v36\n\tv_mfma_f32_32x32x16_bf16 v[16:31], v[144:147], v[80:83], 0\n\tv_exp_f32 v37, v37\n\tv_exp_f32 v38, v38\n\tv_exp_f32 v39, v39\n\t" \
    "v_exp_f32 v40, v40\n\tv_exp_f32 v41, v41\n\tv_exp_f32 v42, v42\n\tv_smfmac_f32_32x32x32_bf16 v[48:63], v[184:187], v[32:39], v94\n\tv_exp_f32 v43, v43\n\tv_exp_f32 v44, v44\n\t" \
    "v_exp_f32 v45, v45\n\tv_exp_f32 v46, v46\n\tv_exp_f32 v47, v47\n\tv_exp_f32 v16, v16\n\tv_exp_f32 v17, v17\n\ts_mov_b32 m0, %[wd]\n\ts_nop 0\n\t" \
    "buffer_load_dword v96, %[rs], %[ws] offen sc1 lds\n\tv_smfmac_f32_32x32x32_bf16 v[48:63], v[188:191], v[40:47], v94\n\ts_waitcnt lgkmcnt(0)\n\ts_barrier\n\t" \
    "ds_read_b128 v[64:67], v90 offset:18432\n\tds_read_b128 v[68:71], v90 offset:19456\n\tds_read_b32 v72, v91 offset:20480\n\ts_add_u32 %[qo], %[qo], 0x400\n\ts_and_b32 %[qo], %[qo], 0x1fff\n\t" \
    "v_add_u32 v97, %[qo], v98\n\tds_read_b128 v[84:87], v97\n\tv_exp_f32 v18, v18\n\tv_exp_f32 v19, v19\n\tv_exp_f32 v20, v20\n\tv_mfma_f32_32x32x16_bf16 v[32:47], v[148:151], v[80:83], 0\n\t" \
    "v_exp_f32 v21, v21\n\tv_exp_f32 v22, v22\n\tv_exp_f32 v23, v23\n\tv_exp_f32 v24, v24\n\tv_exp_f32 v25, v25\n\tv_exp_f32 v26, v26\n\t" \
    "v_smfmac_f32_32x32x32_bf16 v[48:63], v[192:195], v[16:23], v94\n\tv_exp_f32 v27, v27\n\tv_exp_f32 v28, v28\n\tv_exp_f32 v29, v29\n\tv_exp_f32 v30, v30\n\tv_exp_f32 v31, v31\n\t" \
    "v_exp_f32 v32, v32\n\tv_exp_f32 v33, v33\n\tv_smfmac_f32_32x32x32_bf16 v[48:63], v[196:199], v[24:31], v94\n\tv_exp_f32 v34, v34\n\tv_exp_f32 v35, v35\n\tv_exp_f32 v36, v36\n\t" \
    "v_mfma_f32_32x32x16_bf16 v[16:31], v[152:155], v[80:83], 0\n\tv_exp_f32 v37, v37\n\tv_exp_f32 v38, v38\n\tv_exp_f32 v39, v39\n\tv_exp_f32 v40, v40\n\tv_exp_f32 v41, v41\n\t" \
    "v_exp_f32 v42, v42\n\tv_smfmac_f32_32x32x32_bf16 v[48:63], v[200:203], v[32:39], v94\n\tv_exp_f32 v43, v43\n\tv_exp_f32 v44, v44\n\tv_exp_f32 v45, v45\n\tv_exp_f32 v46, v46\n\t" \
    "v_exp_f32 v47, v47\n\tv_exp_f32 v16, v16\n\tv_exp_f32 v17, v17\n\ts_mov_b32 m0, %[pd]\n\ts_nop 0\n\tbuffer_load_dwordx4 v95, %[rs], %[ps] offen offset:1024 sc1 lds\n\t" \
    "v_smfmac_f32_32x32x32_bf16 v[48:63], v[204:207], v[40:47], v94\n\tv_exp_f32 v18, v18\n\tv_exp_f32 v19, v19\n\tv_exp_f32 v20, v20\n\t" \
    "v_mfma_f32_32x32x16_bf16 v[32:47], v[156:159], v[80:83], 0\n\tv_exp_f32 v21, v21\n\tv_exp_f32 v22, v22\n\tv_exp_f32 v23, v23\n\tv_exp_f32 v24, v24\n\tv_exp_f32 v25, v25\n\t" \
    "v_exp_f32 v26, v26\n\tv_smfmac_f32_32x32x32_bf16 v[48:63], v[208:211], v[16:23], v94\n\tv_exp_f32 v27, v27\n\tv_exp_f32 v28, v28\n\tv_exp_f32 v29, v29\n\tv_exp_f32 v30, v30\n\t" \
    "v_exp_f32 v31, v31\n\tv_exp_f32 v32, v32\n\tv_exp_f32 v33, v33\n\tv_smfmac_f32_32x32x32_bf16 v[48:63], v[212:215], v[24:31], v94\n\tv_exp_f32 v34, v34\n\tv_exp_f32 v35, v35\n\t" \
    "v_exp_f32 v36, v36\n\ts_waitcnt lgkmcnt(0)\n\tv_mfma_f32_32x32x16_bf16 v[16:31], v[128:131], v[84:87], 0\n\tv_exp_f32 v37, v37\n\tv_exp_f32 v38, v38\n\tv_exp_f32 v39, v39\n\t" \
    "v_exp_f32 v40, v40\n\tv_exp_f32 v41, v41\n\tv_exp_f32 v42, v42\n\tv_smfmac_f32_32x32x32_bf16 v[48:63], v[216:219], v[32:39], v94\n\tv_exp_f32 v43, v43\n\tv_exp_f32 v44, v44\n\t" \
    "v_exp_f32 v45, v45\n\tv_exp_f32 v46, v46\n\tv_exp_f32 v47, v47\n\tv_exp_f32 v16, v16\n\tv_exp_f32 v17, v17\n\tv_smfmac_f32_32x32x32_bf16 v[48:63], v[220:223], v[40:47], v94\n\t" \
    "v_exp_f32 v18, v18\n\tv_exp_f32 v19, v19\n\tv_exp_f32 v20, v20\n\tv_mfma_f32_32x32x16_bf16 v[32:47], v[132:135], v[84:87], 0\n\tv_exp_f32 v21, v21\n\tv_exp_f32 v22, v22\n\t" \
    "v_exp_f32 v23, v23\n\tv_exp_f32 v24, v24\n\tv_exp_f32 v25, v25\n\tv_exp_f32 v26, v26\n\tv_smfmac_f32_32x32x32_bf16 v[64:79], v[160:163], v[16:23], v94\n\tv_exp_f32 v27, v27\n\t" \
    "v_exp_f32 v28, v28\n\tv_exp_f32 v29, v29\n\tv_exp_f32 v30, v30\n\tv_exp_f32 v31, v31\n\tv_exp_f32 v32, v32\n\tv_exp_f32 v33, v33\n\t" \
    "v_smfmac_f32_32x32x32_bf16 v[64:79], v[164:167], v[24:31], v94\n\tds_write_b128 v88, v[48:51] offset:0\n\tds_write_b128 v88, v[52:55] offset:1024\n\tds_write_b32 v89, v56 offset:2048\n\t" \
    "v_exp_f32 v34, v34\n\tv_exp_f32 v35, v35\n\tv_exp_f32 v36, v36\n\tv_mfma_f32_32x32x16_bf16 v[16:31], v[136:139], v[84:87], 0\n\tv_exp_f32 v37, v37\n\tv_exp_f32 v38, v38\n\t" \
    "v_exp_f32 v39, v39\n\tv_exp_f32 v40, v40\n\tv_exp_f32 v41, v41\n\tv_exp_f32 v42, v42\n\tv_smfmac_f32_32x32x32_bf16 v[64:79], v[168:171], v[32:39], v94\n\tv_exp_f32 v43, v43\n\t" \
    "v_exp_f32 v44, v44\n\tv_exp_f32 v45, v45\n\tv_exp_f32 v46, v46\n\tv_exp_f32 v47, v47\n\tv_exp_f32 v16, v16\n\tv_exp_f32 v17, v17\n\ts_mov_b32 m0, %[pd]\n\ts_nop 0\n\t" \
    "buffer_load_dwordx4 v95, %[rs], %[ps] offen offset:2048 sc1 lds\n\tv_smfmac_f32_32x32x32_bf16 v[64:79], v[172:175], v[40:47], v94\n\tv_exp_f32 v18, v18\n\tv_exp_f32 v19, v19\n\t" \
    "v_exp_f32 v20, v20\n\tv_mfma_f32_32x32x16_bf16 v[32:47], v[140:143], v[84:87], 0\n\tv_exp_f32 v21, v21\n\tv_exp_f32 v22, v22\n\tv_exp_f32 v23, v23\n\tv_exp_f32 v24, v24\n\t" \
    "v_exp_f32 v25, v25\n\tv_exp_f32 v26, v26\n\tv_smfmac_f32_32x32x32_bf16 v[64:79], v[176:179], v[16:23], v94\n\tv_exp_f32 v27, v27\n\tv_exp_f32 v28, v28\n\tv_exp_f32 v29, v29\n\t" \
    "v_exp_f32 v30, v30\n\tv_exp_f32 v31, v31\n\tv_exp_f32 v32, v32\n\tv_exp_f32 v33, v33\n\tv_smfmac_f32_32x32x32_bf16 v[64:79], v[180:183], v[24:31], v94\n\tv_exp_f32 v34, v34\n\t" \
    "v_exp_f32 v35, v35\n\tv_exp_f32 v36, v36\n\tv_mfma_f32_32x32x16_bf16 v[16:31], v[144:147], v[84:87], 0\n\tv_exp_f32 v37, v37\n\tv_exp_f32 v38, v38\n\tv_exp_f32 v39, v39\n\t" \
    "v_exp_f32 v40, v40\n\tv_exp_f32 v41, v41\n\tv_exp_f32 v42, v42\n\tv_smfmac_f32_32x32x32_bf16 v[64:79], v[184:187], v[32:39], v94\n\tv_exp_f32 v43, v43\n\tv_exp_f32 v44, v44\n\t" \
    "v_exp_f32 v45, v45\n\tv_exp_f32 v46, v46\n\tv_exp_f32 v47, v47\n\tv_exp_f32 v16, v16\n\tv_exp_f32 v17, v17\n\tv_smfmac_f32_32x32x32_bf16 v[64:79], v[188:191], v[40:47], v94\n\t" \
    "s_waitcnt lgkmcnt(0)\n\ts_barrier\n\tds_read_b128 v[48:51], v92 offset:0\n\tds_read_b128 v[52:55], v92 offset:1024\n\tds_read_b32 v56, v93 offset:2048\n\ts_add_u32 %[qo], %[qo], 0x400\n\t" \
    "s_and_b32 %[qo], %[qo], 0x1fff\n\tv_add_u32 v97, %[qo], v98\n\tds_read_b128 v[80:83], v97\n\tv_exp_f32 v18, v18\n\tv_exp_f32 v19, v19\n\tv_exp_f32 v20, v20\n\t" \
    "v_mfma_f32_32x32x16_bf16 v[32:47], v[148:151], v[84:87], 0\n\tv_exp_f32 v21, v21\n\tv_exp_f32 v22, v22\n\tv_exp_f32 v23, v23\n\tv_exp_f32 v24, v24\n\tv_exp_f32 v25, v25\n\t" \
    "v_exp_f32 v26, v26\n\tv_smfmac_f32_32x32x32_bf16 v[64:79], v[192:195], v[16:23], v94\n\tv_exp_f32 v27, v27\n\tv_exp_f32 v28, v28\n\tv_exp_f32 v29, v29\n\tv_exp_f32 v30, v30\n\t" \
    "v_exp_f32 v31, v31\n\tv_exp_f32 v32, v32\n\tv_exp_f32 v33, v33\n\tv_smfmac_f32_32x32x32_bf16 v[64:79], v[196:199], v[24:31], v94\n\tv_exp_f32 v34, v34\n\tv_exp_f32 v35, v35\n\t" \
    "v_exp_f32 v36, v36\n\tv_mfma_f32_32x32x16_bf16 v[16:31], v[152:155], v[84:87], 0\n\tv_exp_f32 v37, v37\n\tv_exp_f32 v38, v38\n\tv_exp_f32 v39, v39\n\tv_exp_f32 v40, v40\n\t" \
    "v_exp_f32 v41, v41\n\tv_exp_f32 v42, v42\n\tv_smfmac_f32_32x32x32_bf16 v[64:79], v[200:203], v[32:39], v94\n\tv_exp_f32 v43, v43\n\tv_exp_f32 v44, v44\n\tv_exp_f32 v45, v45\n\t" \
    "v_exp_f32 v46, v46\n\tv_exp_f32 v47, v47\n\tv_exp_f32 v16, v16\n\tv_exp_f32 v17, v17\n\ts_mov_b32 m0, %[pd]\n\ts_nop 0\n\tbuffer_load_dwordx4 v95, %[rs], %[ps] offen offset:3072 sc1 lds\n\t" \
    "v_smfmac_f32_32x32x32_bf16 v[64:79], v[204:207], v[40:47], v94\n\tv_exp_f32 v18, v18\n\tv_exp_f32 v19, v19\n\tv_exp_f32 v20, v20\n\t" \
    "v_mfma_f32_32x32x16_bf16 v[32:47], v[156:159], v[84:87], 0\n\tv_exp_f32 v21, v21\n\tv_exp_f32 v22, v22\n\tv_exp_f32 v23, v23\n\tv_exp_f32 v24, v24\n\tv_exp_f32 v25, v25\n\t" \
    "v_exp_f32 v26, v26\n\tv_smfmac_f32_32x32x32_bf16 v[64:79], v[208:211], v[16:23], v94\n\tv_exp_f32 v27, v27\n\tv_exp_f32 v28, v28\n\tv_exp_f32 v29, v29\n\tv_exp_f32 v30, v30\n\t" \
    "v_exp_f32 v31, v31\n\tv_exp_f32 v32, v32\n\tv_exp_f32 v33, v33\n\tv_smfmac_f32_32x32x32_bf16 v[64:79], v[212:215], v[24:31], v94\n\tv_exp_f32 v34, v34\n\tv_exp_f32 v35, v35\n\t" \
    "v_exp_f32 v36, v36\n\ts_waitcnt lgkmcnt(0)\n\tv_mfma_f32_32x32x16_bf16 v[16:31], v[128:131], v[80:83], 0\n\tv_exp_f32 v37, v37\n\tv_exp_f32 v38, v38\n\tv_exp_f32 v39, v39\n\t" \
    "v_exp_f32 v40, v40\n\tv_exp_f32 v41, v41\n\tv_exp_f32 v42, v42\n\tv_smfmac_f32_32x32x32_bf16 v[64:79], v[216:219], v[32:39], v94\n\tv_exp_f32 v43, v43\n\tv_exp_f32 v44, v44\n\t" \
    "v_exp_f32 v45, v45\n\tv_exp_f32 v46, v46\n\tv_exp_f32 v47, v47\n\tv_exp_f32 v16, v16\n\tv_exp_f32 v17, v17\n\tv_smfmac_f32_32x32x32_bf16 v[64:79], v[220:223], v[40:47], v94\n\ts_nop 15\n\t"

DI void attention_true_max(unsigned w, unsigned lane, const u32x32& kk, const u32x32& va, const u32x32& vb, f32x16& o) {
    auto kf = [&](int t) AI { return u32x4{kk[4 * t], kk[4 * t + 1], kk[4 * t + 2], kk[4 * t + 3]}; };
    auto vf = [&](int i) AI { const u32x32& v = i < 8 ? va : vb; const int b = 4 * (i & 7); return u32x4{v[b], v[b + 1], v[b + 2], v[b + 3]}; };
#pragma unroll 1
    for (unsigned j = 0; j < 8; ++j) {
        const u32x4 qf = lds_ld<u32x4>(L_Q + j * 1024 + lane * 16);
        float m = -__builtin_inff();
#pragma unroll
        for (int t = 0; t < 8; ++t) {
            const f32x16 s = mfma32(kf(t), qf, f32x16{});
#pragma unroll
            for (int e = 0; e < 16; ++e) m = fmaxf(m, s[e]);
        }
        m = swap32_max(m);
        if (lane < 32) lds_st<float>(L_MX + ((w * 8 + j) * 32 + lane) * 4, m);
    }
    bar_lds();
#pragma unroll 1
    for (unsigned j = 0; j < 8; ++j) {
        const u32x4 qf = lds_ld<u32x4>(L_Q + j * 1024 + lane * 16);
        float m = -__builtin_inff();
#pragma unroll
        for (int i = 0; i < 8; ++i) m = fmaxf(m, lds_ld<float>(L_MX + ((i * 8 + j) * 32 + (lane & 31)) * 4));
        f32x16 ci, acc = {};
#pragma unroll
        for (int e = 0; e < 16; ++e) ci[e] = -m;
#pragma unroll
        for (int t = 0; t < 8; ++t) {
            f32x16 s = mfma32(kf(t), qf, ci);
#pragma unroll
            for (int e = 0; e < 16; ++e) s[e] = __builtin_amdgcn_exp2f(s[e]);
            acc = smfmac(vf(2 * t), __builtin_shufflevector(s, s, 0, 1, 2, 3, 4, 5, 6, 7), acc, 0xDDDD);
            acc = smfmac(vf(2 * t + 1), __builtin_shufflevector(s, s, 8, 9, 10, 11, 12, 13, 14, 15), acc, 0xDDDD);
        }
#pragma unroll
        for (int e = 0; e < 9; ++e) lds_st<float>(L_CH + w * 2304 + e * 256 + lane * 4, acc[e]);
        bar_lds();
        if (j == ((w + 7) & 7)) {
#pragma unroll
            for (int e = 0; e < 9; ++e) {
                float s = 0.f;
#pragma unroll
                for (int i = 0; i < 8; ++i) s += lds_ld<float>(L_CH + i * 2304 + e * 256 + lane * 4);
                o[e] = s;
            }
        }
        bar_lds();
    }
}

DI void attention(const Ctx& c0, unsigned layer) {
    const Ctx c = fresh(c0); const unsigned lane = lane_id();
    const unsigned w = c.w, head = 4 * c.g() + (c.r() & 3), qb = c.r() >> 2, t = lane & 31, h = lane >> 5;
    u32x32 kk, va, vb;
    f32x16 oa, ob, sa, sb;
    u32x4 qa, qn;
    u32x8 fw;
    asm volatile(ATT_FILL : "={v[160:191]}"(va), "={v[192:223]}"(vb), "={v[48:63]}"(oa), "={v[64:79]}"(ob) : [cv] "v"((t == 16 || t == 20) ? ONES : 0u));
    if (w == 0 && lane == 0) lds_st<unsigned>(L_VOTE, 0u);
    const unsigned l16 = lane * 16, l4 = lane * 4;
    const unsigned wr16 = L_CH + w * 2304 + l16, wr4 = L_CH + w * 2304 + l4;
    const unsigned rd16 = L_CH + ((w + 2) & 7) * 2304 + l16, rd4 = L_CH + ((w + 2) & 7) * 2304 + l4;
    const unsigned hso = layer * 2 * MB + head * 65536;
    unsigned sk = OFF_K + hso + w * 8192, sv = OFF_V + hso + w * 8192, qo = w * 1024, qtmp;
    wait_flags(c.ws, OFF_FQKV + layer * 8192 + (head >> 1) * 512 + ((lane & 8) ? 8 * qb + w : 8 * w + (lane & 7)) * 8, c.tag);
    STAMP(16 + layer * 16);
    asm volatile(ATT_FIRST
                 : "={v[128:159]}"(kk), "+{v[160:191]}"(va), "+{v[192:223]}"(vb), "+{v[48:63]}"(oa), "+{v[64:79]}"(ob), "={v[16:31]}"(sa), "={v[32:47]}"(sb),
                   "={v[80:83]}"(qa), "={v[84:87]}"(qn), "={v97}"(qtmp), "={v[100:107]}"(fw), [qo] "+s"(qo), [sk] "+s"(sk), [sv] "+s"(sv)
                 : "{v88}"(wr16), "{v89}"(wr4), "{v90}"(rd16), "{v91}"(rd4), "{v92}"(rd16), "{v93}"(rd4), "{v94}"(0xDDDD), "{v95}"(l16), "{v96}"(h * 256 + t * 16),
                   "{v98}"(L_Q + l16), "{v108}"(lane * 8), [rs] "s"(c.rws), [sq] "s"(OFF_Q + hso + (8 * qb + w) * 1024), [qd] "s"(L_Q + w * 1024), [sfw] "s"(OFF_FW),
                   [vm] "s"(0x0000ffff0000ffffull)
                 : "m0", "scc", "memory");
    STAMP(17 + layer * 16);
    if (layer == 0) {
        const unsigned tl = (unsigned)c.tag, th = (unsigned)(c.tag >> 32);
        if (!__all(fw[0] == tl && fw[1] == th && fw[2] == tl && fw[3] == th && fw[4] == tl && fw[5] == th && fw[6] == tl && fw[7] == th)) {
#pragma unroll 1
            for (int q = 0; q < 4; ++q) wait_flags(c.ws, OFF_FW + q * 512 + lane * 8, c.tag);
        }
        asm volatile("" ::: "memory");
    }
#pragma unroll 1
    for (unsigned part = 0; part < 3; ++part) {
        const unsigned src = L_CH + ((w + (part == 2 ? 1 : 2)) & 7) * 2304, wi = c.r() * 8 + w;
        const unsigned ps = part < 2 ? OFF_WO + c.fp() * 65536 + w * 8192 + part * 4096 : OFF_WG + c.col8() * 229376 + w * 4096;
        const unsigned pd = part < 2 ? w * 8192 + part * 4096 : 2 * L_SLOT + w * 4096;
        const unsigned wsrc = part < 2 ? OFF_WG + (c.b & 1) * (8 * 229376) + wi * 7168 + part * 3072 : OFF_WD + (c.b & 1) * (4 * 229376) + wi * 3584;
        asm volatile(ATT_LOOP
                     : "+{v[48:63]}"(oa), "+{v[64:79]}"(ob), "+{v[16:31]}"(sa), "={v[32:47]}"(sb), "+{v[80:83]}"(qa), "={v[84:87]}"(qn), "={v97}"(qtmp), [qo] "+s"(qo)
                     : "{v[128:159]}"(kk), "{v[160:191]}"(va), "{v[192:223]}"(vb), "{v88}"(wr16), "{v89}"(wr4), "{v90}"(rd16), "{v91}"(rd4), "{v92}"(src + l16), "{v93}"(src + l4),
                       "{v94}"(0xDDDD), "{v95}"(l16), "{v96}"(lane * 64), "{v98}"(L_Q + l16), [rs] "s"(c.rws), [ps] "s"(ps), [pd] "s"(pd), [ws] "s"(wsrc), [wd] "s"(L_SINK)
                     : "m0", "scc", "memory");
    }
    STAMP(18 + layer * 16);
    f32x16 o;
#pragma unroll
    for (int e = 0; e < 9; ++e) o[e] = oa[e] + ob[e];
    const float mag = fabsf(o[0]) + fabsf(o[1]) + fabsf(o[2]) + fabsf(o[3]) + fabsf(o[4]) + fabsf(o[5]) + fabsf(o[6]) + fabsf(o[7]) + o[8];
    const bool ok = (o[8] >= 9.313225746e-10f) && (mag < __builtin_inff());
    if (!ok) lds_st<unsigned>(L_VOTE, 1u);
    const unsigned tt = 8 * qb + ((w + 7) & 7);
    auto store_o = [&]() AI {
        const float inv = 1.0f / o[8];
        st16<SC1>(u32x4{pk2(o[0] * inv, o[1] * inv), pk2(o[2] * inv, o[3] * inv), pk2(o[4] * inv, o[5] * inv), pk2(o[6] * inv, o[7] * inv)},
                  c.rws, (t >> 4) * 1024 + (t & 15) * 16 + h * 256, OFF_O + layer * 2 * MB + tt * 32768 + (head >> 1) * 2048 + (head & 1) * 512);
    };
    store_o();
    bar_lds();
    if (rfl(lds_ld<unsigned>(L_VOTE))) {
        attention_true_max(w, lane, kk, va, vb, o);
        vmwait<0>();
        store_o();
    }
    set_flag(c.ws, OFF_FO + layer * 16384 + tt * 256 + head * 8, c.tag, lane);
}

DI void chain_help(const Ctx& c0, unsigned layer) {
    const Ctx c = fresh(c0); const unsigned lane = lane_id();
    bar(); issue_chunk<3>(c, lane);
    if (layer == 0 && c.w == 4 && lane < 56) {
        const unsigned vo = (lane >= 28 ? 7168 - 448 : 0) + lane * 16, so = OFF_CSG + c.col8() * 448;
        f32x4 s = {};
#pragma unroll
        for (int kb = 0; kb < 16; ++kb) s += bc<f32x4>(ld16<SC1>(c.rws, vo, so + kb * 14336));
        lds_st<f32x4>(L_CSG + lane * 16, s);
    }
    bar_lds();
    bar();
    vmwait<0>(); bar(); issue_chunk<4>(c, lane); issue_chunk<5>(c, lane);
    if (layer == 0) {
        const unsigned part = c.r() * 4 + (c.w - 4), vo = (lane >> 3) * 1024 + (lane & 7) * 64, so = part * 12288 + (c.b & 1) * 512;
        warm(c.rwqkv, vo, so); warm(c.rwqkv, vo, so + 4096);
        vmwait<9>();
    } else vmwait<7>();
    bar(); issue_chunk<6>(c, lane);
    vmwait<7>(); bar(); issue_chunk<7>(c, lane);
    vmwait<7>(); bar(); issue_chunk<8>(c, lane);
    vmwait<7>(); bar(); issue_chunk<9>(c, lane);
    vmwait<7>(); bar(); issue_chunk<10>(c, lane);
    vmwait<8>(); bar(); issue_chunk<11>(c, lane);
    bar(); issue_chunk<12>(c, lane);
    bar();
    vmwait<8>(); bar(); issue_chunk<13>(c, lane);
    vmwait<8>(); bar(); issue_chunk<14>(c, lane);
    vmwait<8>(); bar(); issue_chunk<15>(c, lane);
    vmwait<8>(); bar(); issue_chunk<16>(c, lane);
    if (layer == 0) {
        const unsigned vw = qkv_w_voff<false>(lane), hw = c.w - 4;
        auto wpart = [&](int i) AI {
#pragma unroll
            for (int e = 0; e < 8; ++e) { const unsigned p = 32 * i + 8 * hw + e; qkv_w_piece<false>(c, vw, p / 12, p % 12, L_SLOT + p * 1024); }
        };
        vmwait<8>(); bar(); wpart(0);
        vmwait<8>(); bar(); wpart(1);
        bar(); wpart(2);
        bar();
        vmwait<0>(); bar();
        bar();
    } else {
        vmwait<8>(); bar();
        vmwait<0>(); bar();
        bar();
        bar();
    }
}

template <int MODE> DI void proj(const Ctx& c0, unsigned layer, void* out) {
    const Ctx c = fresh(c0); const unsigned lane = lane_id();
    constexpr int KS = MODE ? 28 : 8, N0 = MODE ? 10 : 0, TR = MODE ? 14 : 8, R = 5, P = 4;
    const unsigned w = c.w, kh = w >> 1, tt = c.pair() + (w & 1), ft = 2 * c.fp() + kh;
    const unsigned cc = lane & 15, q = lane >> 4, vo = lane * 16;
    u32x4 res[2];
    if (MODE == 0 && layer == 0) {
#pragma unroll
        for (int a = 0; a < 2; ++a) {
            const u32x2 v0 = ld8<0>(c.rx, cc * 1024 + q * 8, tt * 32768 + a * 16384 + ft * 64), v1 = ld8<0>(c.rx, cc * 1024 + q * 8 + 32, tt * 32768 + a * 16384 + ft * 64);
            res[a] = u32x4{v0.x, v0.y, v1.x, v1.y};
        }
    } else {
#pragma unroll
        for (int a = 0; a < 2; ++a) res[a] = ldfrag(c.rws, vo, (MODE ? OFF_X1 + layer * 2 * MB : OFF_X2) + tt * 32768 + ft * 2048, a);
    }
    if (MODE) wait_flags(c.ws, OFF_FH + layer * 8192 + tt * 128 + kh * 64 + (lane & 7) * 8, c.tag);
    else wait_flags(c.ws, OFF_FO + layer * 16384 + tt * 256 + kh * 128 + (lane & 15) * 8, c.tag);
    STAMP((MODE ? 24 : 19) + layer * 16);
    const unsigned tso = MODE ? OFF_H + layer * 7 * MB + (tt * 56 + 28 * kh) * 2048 : OFF_O + layer * 2 * MB + (tt * 16 + 8 * kh) * 2048;
    auto tload = [&](int s, int a) AI {
        if (MODE && s % 7 == 6) {
            const int i = 2 * s + a;
            const u32x2 lo = ld8<SC1>(c.rws, lane * 8 + (i & 3) * 1024, tso + (i >> 2) * 4096), hi = ld8<SC1>(c.rws, lane * 8 + (i & 3) * 1024 + 512, tso + (i >> 2) * 4096);
            return u32x4{lo.x, lo.y, hi.x, hi.y};
        }
        return ldfrag(c.rws, vo, tso, 2 * s + a);
    };
    u32x4 tok[TR][2], wf[R];
#pragma unroll
    for (int s = 0; s < TR; ++s) { tok[s][0] = tload(s, 0); tok[s][1] = tload(s, 1); }
    bar();
    unsigned wa[4] = {vo + kh * 6144, vo + kh * 2048 + 2048, vo + kh * 6144 + 2 * L_SLOT, vo + kh * 2048 + 2048 + 2 * L_SLOT};
    PIN(wa[0]); PIN(wa[1]); PIN(wa[2]); PIN(wa[3]);
    auto wread = [&](int n) AI { const int s = n >> 2, sl = (N0 + (s >> 2)) & 3; return lds_ld<u32x4>(wa[((n >> 1) & 1) + (sl & 2)] + (sl & 1) * L_SLOT + (s & 3) * 8192 + (n & 1) * 1024); };
#pragma unroll
    for (int n = 0; n < P; ++n) wf[n] = wread(n);
    f32x4 acc[4][2] = {};
    static_for<0, 4 * KS>([&](auto ic) AI {
        constexpr int n = decltype(ic)::v, s = n >> 2, i = n & 3;
        if constexpr (n + P < 4 * KS) wf[(n + P) % R] = wread(n + P);
        acc[i][0] = mfma16(wf[n % R], tok[s % TR][0], acc[i][0]); PIN(acc[i][0]);
        acc[i][1] = mfma16(wf[n % R], tok[s % TR][1], acc[i][1]); PIN(acc[i][1]);
        if constexpr (i == 3 && s + TR < KS) { tok[s % TR][0] = tload(s + TR, 0); tok[s % TR][1] = tload(s + TR, 1); }
        SB();
        if constexpr ((n & 15) == 15 && n + 1 < 4 * KS) { bar(); if (MODE == 1) STAMP(56 + n / 16); }
    });
    STAMP((MODE ? 25 : 20) + layer * 16);
#pragma unroll
    for (int i = 0; i < 4; ++i) lds_st<f32x4>(L_EX + w * 4096 + i * 1024 + vo, acc[2 + (i >> 1)][i & 1]);
    bar_lds();
    u32x2 p[2][2];
    float s1[2], s2[2];
#pragma unroll
    for (int a = 0; a < 2; ++a) {
        s1[a] = 0.f; s2[a] = 0.f;
#pragma unroll
        for (int i = 0; i < 2; ++i) {
            const f32x4 y = acc[i][a] + lds_ld<f32x4>(L_EX + (w ^ 2) * 4096 + (2 * i + a) * 1024 + vo) +
                            f32x4{bflo(res[a][2 * i]), bfhi(res[a][2 * i]), bflo(res[a][2 * i + 1]), bfhi(res[a][2 * i + 1])};
            p[a][i] = u32x2{pk2(y[0], y[1]), pk2(y[2], y[3])};
            s1[a] += (y[0] + y[1]) + (y[2] + y[3]);
            s2[a] = fmaf(y[0], y[0], fmaf(y[1], y[1], fmaf(y[2], y[2], fmaf(y[3], y[3], s2[a]))));
        }
    }
    if (MODE == 1 && layer == 1) {
        const rsrc_t ro = mkrsrc(out, SEQ * DM * 2);
#pragma unroll
        for (int a = 0; a < 2; ++a) st16<0>(merge16(p[a][0], p[a][1]), ro, cc * 1024 + (q & 1) * 32 + (q >> 1) * 16, tt * 32768 + a * 16384 + ft * 64);
        return;
    }
#pragma unroll
    for (int a = 0; a < 2; ++a) stfrag(u32x4{p[a][0].x, p[a][0].y, p[a][1].x, p[a][1].y}, c.rws, vo, (MODE ? OFF_X2 : OFF_X1 + layer * 2 * MB) + tt * 32768 + ft * 2048, a);
    const u32x2 x1 = swap16(bc<unsigned>(s1[0]), bc<unsigned>(s1[1])), x2 = swap16(bc<unsigned>(s2[0]), bc<unsigned>(s2[1]));
    const float t1 = swap32_add(bc<float>(x1.x) + bc<float>(x1.y)), t2 = swap32_add(bc<float>(x2.x) + bc<float>(x2.y));
    if (lane < 32) st8<SC1>(u32x2{bc<unsigned>(t1), bc<unsigned>(t2)}, c.rws, lane * 8, (MODE ? OFF_ST2 : OFF_ST1 + layer * 262144) + tt * 4096 + ft * 256);
    set_flag(c.ws, (MODE ? OFF_FX2 : OFF_FX1 + layer * 8192) + tt * 128 + ft * 8, c.tag, lane);
}

DI void gateup(const Ctx& c0, unsigned layer) {
    const Ctx c = fresh(c0); const unsigned lane = lane_id();
    constexpr int TR = 4, R = 6, P = 5;
    const unsigned w = c.w, cj = c.col8(), tt = c.quad() + w, q = lane >> 4, vo = lane * 16;
    wait_flags(c.ws, OFF_FX1 + layer * 8192 + tt * 128 + (lane & 15) * 8, c.tag);
    STAMP(22 + layer * 16);
    const unsigned tso = OFF_X1 + layer * 2 * MB + tt * 32768;
    u32x4 tok[TR][2], wf[R];
    u32x2 sp[8];
#pragma unroll
    for (int s = 0; s < 2 * TR; ++s) tok[s >> 1][s & 1] = ldfrag(c.rws, vo, tso, s);
    load_stats(c, OFF_ST1 + layer * 262144, tt, lane, sp);
    bar();
    unsigned vh = vo + 2 * L_SLOT;
    PIN(vh);
    auto wread = [&](int n) AI { const int sl = (2 + n / 28) & 3; return lds_ld<u32x4>((sl & 2 ? vh : vo) + (sl & 1) * L_SLOT + (n % 28) * 1024); };
#pragma unroll
    for (int n = 0; n < P; ++n) wf[n] = wread(n);
    f32x4 acc[14][2] = {};
    float mu, rs;
    static_for<0, 224>([&](auto ic) AI {
        constexpr int n = decltype(ic)::v, k = n / 14, i = n % 14;
        if constexpr (n + P < 224) wf[(n + P) % R] = wread(n + P);
        acc[i][0] = mfma16(wf[n % R], tok[k % TR][0], acc[i][0]); PIN(acc[i][0]);
        acc[i][1] = mfma16(wf[n % R], tok[k % TR][1], acc[i][1]); PIN(acc[i][1]);
        if constexpr (i == 13 && k + TR < 16) { tok[k % TR][0] = ldfrag(c.rws, vo, tso, 2 * (k + TR)); tok[k % TR][1] = ldfrag(c.rws, vo, tso, 2 * (k + TR) + 1); }
        if constexpr (n == 20) { reduce_stats(sp, mu, rs); asm volatile("" : "+v"(mu), "+v"(rs)); }
        SB();
        if constexpr (n % 28 == 27) { bar(); STAMP(48 + n / 28); }
    });
    STAMP(23 + layer * 16);
    const u32x2 mu2 = swap16(bc<unsigned>(mu), bc<unsigned>(mu)), rr2 = swap16(bc<unsigned>(rs * rs), bc<unsigned>(rs * rs));
    const unsigned ho = OFF_H + layer * 7 * MB + (tt * 56 + 7 * (cj >> 1)) * 2048;
    auto hval = [&](int i, int a) AI {
        const float m = bc<float>(mu2[a]);
        const f32x4 cg = lds_ld<f32x4>(L_CSG + (16 * i + 4 * q) * 4), cu = lds_ld<f32x4>(L_CSG + (112 + 16 * i + 4 * q) * 4);
        const f32x4 y = (acc[2 * i][a] - m * cg) * bc<float>(rr2[a]) * (acc[2 * i + 1][a] - m * cu);
        return u32x2{pk2(y[0], y[1]), pk2(y[2], y[3])};
    };
#pragma unroll
    for (int e = 0; e < 3; ++e)
#pragma unroll
        for (int a = 0; a < 2; ++a) {
            const u32x2 lo = hval(2 * e, a), hi = hval(2 * e + 1, a);
            stfrag(u32x4{lo.x, lo.y, hi.x, hi.y}, c.rws, vo, ho + (cj & 1) * 6144, 2 * e + a);
        }
#pragma unroll
    for (int a = 0; a < 2; ++a) st8<SC1>(hval(6, a), c.rws, lane * 8 + a * 1024, ho + 12288 + (cj & 1) * 512);
    set_flag(c.ws, OFF_FH + layer * 8192 + tt * 128 + cj * 8, c.tag, lane);
}

DI void warm_code(const Ctx& c) {
    u64 pc; unsigned len;
    asm volatile("s_getpc_b64 %0\n.Lfused_code_begin:\n\ts_mov_b32 %1, .Lfused_code_end-.Lfused_code_begin" : "=s"(pc), "=s"(len));
    const unsigned so = (c.r() * 4 + c.w) * 4096;
    if (so < len) warm(mkrsrc((const void*)pc, len), lane_id() * 64, so);
}

__global__ void __launch_bounds__(512, 2) fused_layers(const void* x, const void* wqkv, const void* wo, const void* wg, const void* wu, const void* wd, void* out, char* ws) {
    __shared__ __attribute__((aligned(16))) char lds[LDS_BYTES];
    asm volatile("" ::"v"(lds) : "memory");
    Ctx c;
    c.ws = ws; c.b = blockIdx.x; c.w = rfl(threadIdx.x >> 6);
    c.rws = mkrsrc(ws, WS_USED); c.rx = mkrsrc(x, SEQ * DM * 2); c.rwqkv = mkrsrc(wqkv, DM * 3 * DM * 2);
    STAMP(0);
    if (c.w < 4) {
        warm_code(c);
        const u64 e = __hip_atomic_load((gu64*)(ws + OFF_EPOCH), RLX_AGENT) + 1;
        c.tag = ((u64)rfl((unsigned)(e >> 32)) << 32) | rfl((unsigned)e);
        if (c.w == 0 && lane_id() == 0) lds_st<u64>(L_VOTE + 8, c.tag);
        asm volatile("s_waitcnt lgkmcnt(0)" ::: "memory");
        phaseA_mfma(c);
    } else {
        phaseA_help(c, wo, wg, wu, wd);
        const u64 e = lds_ld<u64>(L_VOTE + 8);
        c.tag = ((u64)rfl((unsigned)(e >> 32)) << 32) | rfl((unsigned)e);
    }
    STAMP(1);
#pragma unroll 1
    for (unsigned layer = 0; layer < 2; ++layer) {
        attention(c, layer);
        STAMP(2 + layer * 8);
        if (c.w < 4) {
            proj<0>(c, layer, out);
            STAMP(3 + layer * 8);
            gateup(c, layer);
            STAMP(4 + layer * 8);
            proj<1>(c, layer, out);
            STAMP(5 + layer * 8);
            if (layer == 0) phaseF(c);
            STAMP(6 + layer * 8);
        } else {
            chain_help(c, layer);
        }
    }
    STAMP_OUT();
    asm volatile(".Lfused_code_end:");
    if (c.b == 0 && c.w == 0 && lane_id() == 0) __hip_atomic_store((gu64*)(ws + OFF_EPOCH), c.tag, RLX_AGENT);
}

}

extern "C" void kernel_launch(void* const* d_in, const int* in_sizes, int n_in, void* d_out, int out_size, void* d_ws, size_t ws_size, hipStream_t stream) {
    (void)in_sizes; (void)n_in; (void)out_size; (void)ws_size;
    fused_layers<<<256, 512, 0, stream>>>(d_in[0], d_in[1], d_in[2], d_in[4], d_in[5], d_in[6], d_out, (char*)d_ws);
}
```

```cpp
#include <hip/hip_runtime.h>
#include <hip/hip_bf16.h>
#include <stdint.h>

#pragma clang diagnostic ignored "-Winline-asm"

typedef __attribute__((ext_vector_type(2))) unsigned u32x2;
typedef __attribute__((ext_vector_type(4))) unsigned u32x4;
typedef __attribute__((ext_vector_type(8))) unsigned u32x8;
typedef __attribute__((ext_vector_type(32))) unsigned u32x32;
typedef __attribute__((ext_vector_type(4))) float f32x4;
typedef __attribute__((ext_vector_type(8))) float f32x8;
typedef __attribute__((ext_vector_type(16))) float f32x16;
typedef __attribute__((ext_vector_type(4))) short s16x4;
typedef __bf16 bf16x2 __attribute__((ext_vector_type(2)));
typedef __bf16 bf16x8 __attribute__((ext_vector_type(8)));
typedef __bf16 bf16x16 __attribute__((ext_vector_type(16)));
typedef __amdgpu_buffer_rsrc_t rsrc_t;
typedef unsigned long long u64;
typedef __attribute__((address_space(1))) u64 gu64;
#define LAS __attribute__((address_space(3)))
#define DI __device__ __forceinline__
#define AI __attribute__((always_inline))
#define SB() __builtin_amdgcn_sched_barrier(0)
#define PIN(x) asm volatile("" : "+v"(x))
#define RLX_AGENT __ATOMIC_RELAXED, __HIP_MEMORY_SCOPE_AGENT

#if defined(__HIP_DEVICE_COMPILE__)
#pragma clang attribute push(__attribute__((target("no-packed-fp32-ops"))), apply_to = function)
#endif

#define XSPLIT 2
#define WARM_DELAY 12
#define TILE_DELAY 16
#define PACE3 1
#define POLL_DELAY 0
#define PROBE_AT 200
#define EARLY 4
#define PLACE_PACE 0
#define SPACING 2
#define I0E 6
#define I0C 8
#define I0D 4
#define SPD 3
#define I0F 7
#define SPF 2
#define WARM_WQ 1
#define A_START 3
#define PACE2 1
#define PACE 3

namespace {

constexpr int SEQ = 2048, DM = 512, FF = 1792;
constexpr int SC1 = 16;
constexpr unsigned ONES = 0x3f803f80u, EMPTY = 0xffffffffu, SPIN_CAP = 1u << 18;

constexpr unsigned MB = 1u << 20;
constexpr unsigned OFF_EPOCH = 0;
constexpr unsigned OFF_FQKV = 4096;
constexpr unsigned OFF_FW = OFF_FQKV + 2 * 16 * 128 * 8;
constexpr unsigned OFF_CSG = 64 * 1024;
constexpr unsigned OFF_CSQ = OFF_CSG + 16 * 14336;
constexpr unsigned OFF_ST1 = 512 * 1024;
constexpr unsigned OFF_ST2 = OFF_ST1 + 2 * 256 * 1024;
constexpr unsigned OFF_WO = OFF_ST2 + 256 * 1024;
constexpr unsigned OFF_WD = OFF_WO + 512 * 1024;
constexpr unsigned OFF_WG = OFF_WD + 1792 * 1024;
constexpr unsigned OFF_WQ = 46 * MB;
constexpr unsigned OFF_Q = 8 * MB;
constexpr unsigned OFF_K = OFF_Q + 4 * MB;
constexpr unsigned OFF_V = OFF_K + 4 * MB;
constexpr unsigned OFF_O = OFF_V + 4 * MB;
constexpr unsigned OFF_X1 = OFF_O + 4 * MB;
constexpr unsigned OFF_X2 = OFF_X1 + 4 * MB;
constexpr unsigned OFF_H = OFF_X2 + 2 * MB;
constexpr unsigned OFF_DBG = OFF_H + 14 * MB;
constexpr unsigned WS_USED = OFF_WQ + 2 * MB;
static_assert(OFF_WG + 3584 * 1024 <= OFF_Q && OFF_CSQ + 16 * 6144 <= OFF_ST1 && OFF_DBG + 2 * MB <= OFF_WQ, "workspace map");

constexpr unsigned L_SLOT = 32768;
constexpr unsigned L_V = 65536;
constexpr unsigned L_EX = 131072;
constexpr unsigned L_PART = L_EX + 4096;
constexpr unsigned L_RES = L_EX + 16384;
constexpr unsigned L_CSP = L_PART + 8 * 2304;
constexpr unsigned L_DBG = 157568;
constexpr unsigned L_CSQ = L_DBG + 3072;
constexpr unsigned L_MU = L_CSQ + 768;
constexpr unsigned L_RS = L_MU + 512;
constexpr unsigned L_CSG = L_RS + 512;
constexpr unsigned L_SYNC = L_CSG + 896;
constexpr unsigned L_SINK = L_SYNC + 256;
constexpr unsigned LDS_BYTES = 163840;
static_assert(L_CSP + 3968 <= L_DBG && L_RES + 8192 <= L_DBG && L_SINK + 256 <= LDS_BYTES, "LDS map");

template <int I> struct IC { static constexpr int v = I; };
template <int B, int E, class Fn> DI void static_for(Fn&& f) {
    if constexpr (E - B == 1) f(IC<B>{});
    else if constexpr (E - B > 1) { static_for<B, (B + E) / 2>(f); static_for<(B + E) / 2, E>(f); }
}
template <class T, class U> DI T bc(U v) { return __builtin_bit_cast(T, v); }
DI unsigned umax(unsigned a, unsigned b) { return a > b ? a : b; }
DI unsigned umin(unsigned a, unsigned b) { return a < b ? a : b; }
DI bool all_lanes(bool p) { return __builtin_amdgcn_ballot_w64(!p) == 0; }
DI unsigned rfl(unsigned v) { return __builtin_amdgcn_readfirstlane(v); }
DI unsigned lane_id() { unsigned l; asm volatile("v_mbcnt_lo_u32_b32 %0, -1, 0\n\tv_mbcnt_hi_u32_b32 %0, -1, %0" : "=v"(l)); return l; }
template <unsigned V> DI unsigned vconst() { unsigned r; asm volatile("v_mov_b32 %0, %1" : "=v"(r) : "i"(V)); return r; }
DI rsrc_t mkrsrc(const void* p, unsigned bytes) { return __builtin_amdgcn_make_buffer_rsrc((void*)p, (short)0, (int)bytes, 0x00020000); }
DI f32x16 mfma32(u32x4 a, u32x4 b, f32x16 c) { return __builtin_amdgcn_mfma_f32_32x32x16_bf16(bc<bf16x8>(a), bc<bf16x8>(b), c, 0, 0, 0); }
DI f32x4 mfma16(u32x4 a, u32x4 b, f32x4 c) { return __builtin_amdgcn_mfma_f32_16x16x32_bf16(bc<bf16x8>(a), bc<bf16x8>(b), c, 0, 0, 0); }
template <bool FIRST> DI void mma(f32x4& c, u32x4 a, u32x4 b) {
    if constexpr (FIRST) asm volatile("v_mfma_f32_16x16x32_bf16 %0, %1, %2, 0" : "=v"(c) : "v"(a), "v"(b));
    else asm volatile("v_mfma_f32_16x16x32_bf16 %0, %1, %2, %0" : "+v"(c) : "v"(a), "v"(b));
}
DI void settle() { SB(); asm volatile("s_nop 7"); SB(); }
DI f32x16 smfmac(u32x4 a, f32x8 b, f32x16 c, int idx) { return __builtin_amdgcn_smfmac_f32_32x32x32_bf16(bc<bf16x8>(a), bc<bf16x16>(b), c, idx, 0, 0); }
DI unsigned pk2(float lo, float hi) { bf16x2 v = {(__bf16)lo, (__bf16)hi}; return bc<unsigned>(v); }
DI float bflo(unsigned d) { return bc<float>(d << 16); }
DI float bfhi(unsigned d) { return bc<float>(d & 0xffff0000u); }
DI u32x2 swap32(unsigned a, unsigned b) { auto r = __builtin_amdgcn_permlane32_swap(a, b, false, false); return u32x2{r[0], r[1]}; }
DI u32x2 swap16(unsigned a, unsigned b) { auto r = __builtin_amdgcn_permlane16_swap(a, b, false, false); return u32x2{r[0], r[1]}; }
DI float swap32_add(float v) { const u32x2 r = swap32(bc<unsigned>(v), bc<unsigned>(v)); return bc<float>(r.x) + bc<float>(r.y); }
DI float swap32_max(float v) { const u32x2 r = swap32(bc<unsigned>(v), bc<unsigned>(v)); return __builtin_fmaxf(bc<float>(r.x), bc<float>(r.y)); }
DI u32x4 merge16(u32x2 a, u32x2 b) { const u32x2 x = swap16(a.x, b.x), y = swap16(a.y, b.y); return u32x4{x.x, y.x, x.y, y.y}; }

template <class T> DI T lds_ld(unsigned a) { return *(const LAS T*)(size_t)a; }
template <class T> DI void lds_st(unsigned a, T v) { *(LAS T*)(size_t)a = v; }
DI u32x4 lds_tr(unsigned a, unsigned step) {
    const u32x2 lo = bc<u32x2>(__builtin_amdgcn_ds_read_tr16_b64_v4i16((LAS s16x4*)(size_t)a));
    const u32x2 hi = bc<u32x2>(__builtin_amdgcn_ds_read_tr16_b64_v4i16((LAS s16x4*)(size_t)(a + step)));
    return u32x4{lo.x, lo.y, hi.x, hi.y};
}

template <int AUX> DI u32x4 ld16(rsrc_t r, unsigned vo, unsigned so) { return __builtin_amdgcn_raw_buffer_load_b128(r, vo, so, AUX); }
template <int AUX> DI u32x2 ld8(rsrc_t r, unsigned vo, unsigned so) { return __builtin_amdgcn_raw_buffer_load_b64(r, vo, so, AUX); }
template <int AUX> DI void st16(u32x4 v, rsrc_t r, unsigned vo, unsigned so) {
    __builtin_amdgcn_raw_buffer_store_b128(v, r, vo, so, AUX);
    asm volatile("s_nop 1" ::"v"(v) : "memory");
}
template <int AUX> DI void st8(u32x2 v, rsrc_t r, unsigned vo, unsigned so) { __builtin_amdgcn_raw_buffer_store_b64(v, r, vo, so, AUX); }
DI u32x4 ldfrag(rsrc_t r, unsigned vo, unsigned so, int i) { return ld16<SC1>(r, vo + (i & 3) * 1024, so + (i >> 2) * 4096); }
DI void stfrag(u32x4 v, rsrc_t r, unsigned vo, unsigned so, int i) { st16<SC1>(v, r, vo + (i & 3) * 1024, so + (i >> 2) * 4096); }

template <bool DEV, int IMM> DI void dma(rsrc_t r, unsigned vo, unsigned so, unsigned la) {
    if constexpr (DEV) asm volatile("s_mov_b32 m0, %0\n\ts_nop 0\n\tbuffer_load_dwordx4 %1, %2, %3 offen offset:%4 sc1 lds" ::"s"(la), "v"(vo), "s"(r), "s"(so), "i"(IMM) : "m0", "memory");
    else asm volatile("s_mov_b32 m0, %0\n\ts_nop 0\n\tbuffer_load_dwordx4 %1, %2, %3 offen offset:%4 lds" ::"s"(la), "v"(vo), "s"(r), "s"(so), "i"(IMM) : "m0", "memory");
}
DI void warm(rsrc_t r, unsigned vo, unsigned so) {
    asm volatile("s_mov_b32 m0, %0\n\ts_nop 0\n\tbuffer_load_dword %1, %2, %3 offen sc1 lds" ::"s"(L_SINK), "v"(vo), "s"(r), "s"(so) : "m0", "memory");
}
template <int N> DI void vmwait() { asm volatile("s_waitcnt vmcnt(%0)" ::"i"(N) : "memory"); }
DI void bar() { SB(); asm volatile("s_barrier" ::: "memory"); SB(); }
DI void bar_lds() { SB(); asm volatile("s_waitcnt lgkmcnt(0)\n\ts_barrier" ::: "memory"); SB(); }

DI void wait_flags(char* ws, unsigned off, u64 tag) {
    gu64* p = (gu64*)(ws + off);
    for (unsigned sp = 0; sp < SPIN_CAP; ++sp) {
        const u64 v = __hip_atomic_load(p, RLX_AGENT);
        if (all_lanes(v == tag)) break;
    }
    asm volatile("" ::: "memory");
}
DI void set_flag(char* ws, unsigned off, u64 tag, unsigned lane) {
    vmwait<0>();
    if (lane == 0) __hip_atomic_store((gu64*)(ws + off), tag, RLX_AGENT);
}

DI unsigned max3u(unsigned a, unsigned b, unsigned c) { return umax(umax(a, b), c); }
DI unsigned pub(unsigned d) { return umin(d, EMPTY - 1); }
DI u32x2 pub(u32x2 v) { return u32x2{pub(v.x), pub(v.y)}; }
DI u32x4 pub(u32x4 v) { return u32x4{pub(v.x), pub(v.y), pub(v.z), pub(v.w)}; }
DI bool missing(unsigned m) { return __builtin_amdgcn_ballot_w64(m == EMPTY) != 0; }
DI bool missing(u32x4 a, u32x4 b) { return missing(umax(max3u(max3u(max3u(a.x, a.y, a.z), a.w, b.x), b.y, b.z), b.w)); }
struct Check { unsigned m; u64 bad; };
template <int PIECE, class Ld> DI void ensure(Check& k, u32x4& a, u32x4& b, Ld&& ld) {
    if constexpr (PIECE == 0) { k.m = max3u(max3u(a.x, a.y, a.z), a.w, b.x); PIN(k.m); }
    else if constexpr (PIECE == 1) { k.bad = __builtin_amdgcn_ballot_w64(umax(max3u(k.m, b.y, b.z), b.w) == EMPTY); asm volatile("" : "+s"(k.bad)); }
    else if (__builtin_expect(k.bad != 0, 0)) {
        unsigned sp = 0;
        do { asm volatile("" ::: "memory"); ld(a, b); } while (missing(a, b) && ++sp < SPIN_CAP);
    }
}
DI void await(const rsrc_t& r, unsigned vo, unsigned so) {
    vmwait<0>();
    for (unsigned n = 0; n < SPIN_CAP; ++n) {
        asm volatile("" ::: "memory");
        if (!missing(__builtin_amdgcn_raw_buffer_load_b32(r, vo, so, SC1))) break;
    }
    asm volatile("" ::: "memory");
}

#define STAMP(i)
#define STAMP_OUT()

#define STAMP3(i)
#define STAMP2(i)

struct Ctx {
    char* ws;
    rsrc_t rws, rx, rwqkv;
    u64 tag;
    unsigned b, w;
    DI unsigned g() const { return b & 7; }
    DI unsigned r() const { return b >> 3; }
    DI unsigned col8() const { return 8 * (b & 1) + ((b >> 3) & 7); }
    DI unsigned quad() const { return 16 * ((b & 7) >> 1) + 4 * (b >> 6); }
    DI unsigned fp() const { return 4 * (b & 1) + ((b >> 3) & 3); }
    DI unsigned pair() const { return 16 * ((b & 7) >> 1) + 2 * (b >> 5); }
    DI unsigned half() const { return b & 1; }
};
DI Ctx fresh(const Ctx& c0) { Ctx c = c0; asm volatile("" : "+s"(c.b), "+s"(c.w), "+s"(c.tag)); return c; }

DI void finish_stats(float s, float q, float& mu, float& rs) {
    mu = s * (1.f / DM);
    rs = __builtin_amdgcn_rsqf(__builtin_fmaxf(q * (1.f / DM) - mu * mu, 0.f) + 1e-5f);
}
DI void load_stats(const Ctx& c, unsigned off, unsigned tt, unsigned lane, u32x2 (&sp)[8]) {
    const unsigned vo = (lane & 31) * 8 + (lane >> 5) * 2048;
#pragma unroll
    for (int i = 0; i < 8; ++i) sp[i] = ld8<SC1>(c.rws, vo + i * 256, off + tt * 4096);
}
DI bool missing(const u32x2 (&sp)[8]) {
    unsigned m = 0;
#pragma unroll
    for (int i = 0; i < 8; ++i) m = max3u(m, sp[i].x, sp[i].y);
    return missing(m);
}
DI void reduce_stats(const Ctx& c, unsigned off, unsigned tt, unsigned lane, u32x2 (&sp)[8], float& mu, float& rs) {
    if (__builtin_expect(missing(sp), 0)) {
        unsigned n = 0;
        do { asm volatile("" ::: "memory"); load_stats(c, off, tt, lane, sp); } while (missing(sp) && ++n < SPIN_CAP);
    }
    float s = 0.f, q = 0.f;
#pragma unroll
    for (int i = 0; i < 8; ++i) { s += bc<float>(sp[i].x); q += bc<float>(sp[i].y); }
    finish_stats(swap32_add(s), swap32_add(q), mu, rs);
}

struct QkvAcc { f32x4 t[6][2]; };
template <int I, int H, bool FIRST> DI void qkv_mma(QkvAcc& a, u32x4 wf, u32x4 x) {
    if constexpr (I < 4) mma<FIRST>(a.t[I][H], wf, x); else mma<FIRST>(a.t[I][H], x, wf);
    SB();
}
struct QkvNorm { f32x4 cs[4], m4[2], r4[2], u[2]; };
DI void qkv_norm(QkvNorm& n, unsigned lcs, unsigned flip, unsigned w, unsigned lane) {
    const unsigned cc = lane & 15, q = lane >> 4, tl = w * 32;
#pragma unroll
    for (int i = 0; i < 4; ++i) n.cs[i] = lds_ld<f32x4>(lcs + (16 * (i ^ flip) + 4 * q) * 4);
#pragma unroll
    for (int h = 0; h < 2; ++h) {
        const float rs = lds_ld<float>(L_RS + (tl + 16 * h + cc) * 4);
        n.u[h] = f32x4{lds_ld<float>(lcs + (64 + 16 * (h ^ flip) + cc) * 4), lds_ld<float>(L_MU + (tl + 16 * h + cc) * 4), rs, rs * (0.25f * 1.4426950408889634f)};
        n.m4[h] = lds_ld<f32x4>(L_MU + (tl + 16 * h + 4 * q) * 4); n.r4[h] = lds_ld<f32x4>(L_RS + (tl + 16 * h + 4 * q) * 4);
    }
}
struct QkvOut {
    float y[4];
    u32x2 p[2];
    unsigned so, vk;
    DI void init(unsigned layer, unsigned tt, unsigned j, unsigned lane) {
        const unsigned t = lane & 31;
        so = layer * 2 * MB + (2 * j) * 65536 + tt * 1024;
        vk = (((t & 3) | ((t >> 4) << 2) | (((t >> 2) & 3) << 3)) + (lane & 32)) * 16;
    }
    template <int E> DI void piece(const Ctx& c, unsigned flip, unsigned lane, const QkvAcc& a, const QkvNorm& n) {
        constexpr int i = (E / 10 + 2) % 6, h = (E / 5) & 1, r = E % 5;
        if constexpr (r < 4) {
            y[r] = i < 4 ? __builtin_fmaf(-n.u[h][1], n.cs[i & 3][r], a.t[i][h][r]) * n.u[h][i < 2 ? 3 : 2] : __builtin_fmaf(-n.m4[h][r], n.u[i & 1][0], a.t[i][h][r]) * n.r4[h][r];
            PIN(y[r]);
        } else {
            p[h] = u32x2{pk2(y[0], y[1]), pk2(y[2], y[3])}; PIN(p[h]);
            if constexpr (h == 1) {
                const unsigned o = so + ((i & 1) ^ flip) * 65536;
                if constexpr (i >= 4) st16<SC1>(u32x4{p[0].x, p[0].y, p[1].x, p[1].y}, c.rws, lane * 16, OFF_V + o);
                else st16<SC1>(merge16(p[0], p[1]), c.rws, i < 2 ? lane * 16 : vk, (i < 2 ? OFF_Q : OFF_K) + o);
            }
        }
    }
    DI void publish(const Ctx& c, unsigned layer, unsigned tt, unsigned j, unsigned lane) {
        gu64* f = (gu64*)(c.ws + OFF_FQKV + layer * 16384 + j * 1024 + tt * 8);
        vmwait<2>();
        if (lane == 0) __hip_atomic_store(f, c.tag, RLX_AGENT);
        vmwait<1>();
        if (lane == 0) __hip_atomic_store(f + 64, c.tag, RLX_AGENT);
    }
};

DI unsigned qkv_w_lane(unsigned lane, unsigned hp) {
    const unsigned q = lane >> 4, e = (lane & 15) >> 2;
    return 8192 + (4 * q + e) * 128 + (((2 * hp) ^ (2 * (q & 1) + (e >> 1))) * 32) + (lane & 3) * 8;
}
struct Unit { const void* base; unsigned bytes, pitch, src[4], dst[4], cs; };
template <int U> DI Unit make_unit(const Ctx& c, const void* wqkv, const void* wo, const void* wg, const void* wu, const void* wd) {
    const unsigned gh = c.b * 4 + c.w - 4;
    Unit t;
    unsigned ra, rb, col;
    bool swap = false;
    t.cs = WS_USED;
    if (U == 0 && gh < 896) {
        const unsigned m = gh / 448, u = gh % 448, kb = u & 15, cb = u >> 4;
        t.base = m ? wu : wg; t.bytes = DM * FF * 2; t.pitch = FF * 2; ra = 32 * kb; rb = ra + 16; col = 64 * cb;
        t.cs = OFF_CSG + (kb * 2 + m) * 7168 + cb * 256;
#pragma unroll
        for (int ct = 0; ct < 4; ++ct) {
            const unsigned fb = 4 * cb + ct, i = fb % 7, b = i < 2 ? i : i == 6 ? 2 : i + 1;
            t.dst[ct] = OFF_WG + (fb / 7) * 229376 + (kb < 8 ? kb * 14 + 2 * b : 96 + b * 16 + kb * 2) * 1024 + m * 1024;
        }
    } else if (U == 0) {
        const unsigned u = gh - 896, kb = u & 15, cb = u >> 4;
        t.base = wo; t.bytes = DM * DM * 2; t.pitch = DM * 2; ra = 32 * kb; rb = ra + 16; col = 64 * cb; swap = true;
#pragma unroll
        for (int ct = 0; ct < 4; ++ct) t.dst[ct] = OFF_WO + ((cb * 8 + (kb & 7)) * 2 + (kb >> 3)) * 4096 + ct * 1024;
    } else if (gh < 448) {
        const unsigned sg = gh % 56, s = sg % 28, hf = s >= 8 && s < 12, pr = 4 * (sg / 28) + (s < 8 ? s >> 1 : hf ? s - 8 : ((s - 12) & 7) >> 1);
        const unsigned fa = 14 * pr + (hf ? 6 : 7 * (s & 1) + (s < 8 ? 0 : s < 20 ? 2 : 4));
        t.base = wd; t.bytes = FF * DM * 2; t.pitch = DM * 2; ra = 16 * fa; rb = 16 * (fa + (hf ? 7 : 1)); col = 64 * (gh / 56);
#pragma unroll
        for (int ct = 0; ct < 4; ++ct) t.dst[ct] = OFF_WD + (((gh / 56) * 28 + s) * 2 + sg / 28) * 4096 + ct * 1024;
    } else {
        const unsigned u = gh - 448, kb = u & 15, cb = u >> 4, live = gh < 832;
        t.base = wqkv; t.bytes = live ? DM * 3 * DM * 2 : 0; t.pitch = 3072; ra = 32 * kb; rb = ra + 16; col = 64 * cb;
        t.cs = live ? OFF_CSQ + kb * 6144 + cb * 256 : WS_USED;
#pragma unroll
        for (int ct = 0; ct < 4; ++ct)
            t.dst[ct] = live ? OFF_WQ + ((2 * (cb & 7) + (ct >> 1)) * 96 + (cb >> 3 ? kb * 4 + 2 * (cb >> 4) : 64 + kb * 2) + (ct & 1)) * 1024 : WS_USED;
    }
    t.src[0] = ra * t.pitch + col * 2; t.src[3] = (rb + 8) * t.pitch + col * 2;
    t.src[1] = (swap ? rb : ra + 8) * t.pitch + col * 2; t.src[2] = (swap ? ra + 8 : rb) * t.pitch + col * 2;
    asm volatile("" : "+s"(t.base), "+s"(t.bytes), "+s"(t.pitch), "+s"(t.cs), "+s"(t.src[0]), "+s"(t.src[1]), "+s"(t.src[2]), "+s"(t.src[3]),
                 "+s"(t.dst[0]), "+s"(t.dst[1]), "+s"(t.dst[2]), "+s"(t.dst[3]));
    return t;
}
DI void unit_store(const Ctx& c, const Unit& t, unsigned la, unsigned lane, u32x4 ones) {
    u32x4 f[4];
    f32x4 s[4];
#pragma unroll
    for (int ct = 0; ct < 4; ++ct) f[ct] = lds_tr(la + (4 * (lane >> 4) + ((lane & 15) >> 2)) * 128 + (lane & 3) * 8 + ct * 32, 2048);
#pragma unroll
    for (int ct = 0; ct < 4; ++ct) { st16<SC1>(f[ct], c.rws, lane * 16, t.dst[ct]); s[ct] = mfma16(ones, f[ct], f32x4{}); }
    const float sv = (lane & 32) ? ((lane & 16) ? s[3][0] : s[2][0]) : ((lane & 16) ? s[1][0] : s[0][0]);
    __builtin_amdgcn_raw_buffer_store_b32(bc<unsigned>(sv), c.rws, lane * 4, t.cs, SC1);
}

DI void phaseA_mfma(const Ctx& c0) {
    const Ctx c = fresh(c0); const unsigned lane = lane_id();
    constexpr int R = 5, P = 4;
    const unsigned w = c.w, cc = lane & 15, q = lane >> 4;
    unsigned wl[4], xa[4];
    wl[0] = qkv_w_lane(lane, w >> 1) ^ ((w & 1) * 32); wl[1] = wl[0] ^ 32; wl[2] = wl[0] + 2 * L_SLOT; wl[3] = wl[1] + 2 * L_SLOT;
    xa[0] = (32 * (w & 1) + cc) * 128 + ((q ^ (cc >> 1)) * 16); xa[1] = xa[0] ^ 64; xa[2] = xa[0] + 2 * L_SLOT; xa[3] = xa[1] + 2 * L_SLOT;
    unsigned xs[4];
#pragma unroll
    for (int i = 0; i < 4; ++i) { xs[i] = xa[i] + (w >> 1) * 2048; PIN(wl[i]); PIN(xa[i]); PIN(xs[i]); }
    auto whalf = [&](int n, int j) AI {
        const int k = n / 6, i = n % 6, sl = (k >> 1) & 3;
        return bc<u32x2>(__builtin_amdgcn_ds_read_tr16_b64_v4i16((LAS s16x4*)(size_t)(wl[(i & 1) + (sl & 2)] + (sl & 1) * L_SLOT + (i >> 1) * 8192 + (k & 1) * 4096 + j * 2048)));
    };
    auto xread = [&](const unsigned (&base)[4], int k, int h) AI { const int sl = (k >> 1) & 3; return lds_ld<u32x4>(base[(k & 1) + (sl & 2)] + (sl & 1) * L_SLOT + h * 2048); };
    const unsigned one = vconst<ONES>(), emp = vconst<EMPTY>();
    const u32x4 ones = {one, one, one, one}, empty = {emp, emp, emp, emp};
    QkvAcc a;
    f32x4 sx, sq, cs[3];
    u32x2 wf[R][2];
    u32x4 xf[2][2], xt;
    STAMP3(7);
    bar();
    STAMP3(8);
    xf[0][0] = xread(xa, 0, 0); xf[0][1] = xread(xa, 0, 1); xt = xread(xs, 0, 0);
#pragma unroll
    for (int n = 0; n < P; ++n) { wf[n][0] = whalf(n, 0); wf[n][1] = whalf(n, 1); }
    static_for<0, 96>([&](auto ic) AI {
        constexpr int n = decltype(ic)::v, k = n / 6, i = n % 6;
        const u32x4 f = {wf[n % R][0].x, wf[n % R][0].y, wf[n % R][1].x, wf[n % R][1].y};
        qkv_mma<i, 0, k == 0>(a, f, xf[k & 1][0]);
        if constexpr (n + P < 96) wf[(n + P) % R][0] = whalf(n + P, 0);
        if constexpr (i == 3 && k < 15) xt = xread(xs, k + 1, 0);
        if constexpr (n == 94) {
            const float d = (cc & 2) ? ((cc & 1) ? sq[3] : sq[2]) : ((cc & 1) ? sq[1] : sq[0]);
            const unsigned o = (32 * (w & 1) + 16 * (w >> 1) + cc) * 4;
            float mu, rs;
            finish_stats(sx[0], d, mu, rs);
            if (q == (cc >> 2)) { lds_st<float>(L_MU + o, mu); lds_st<float>(L_RS + o, rs); }
        }
        SB();
        qkv_mma<i, 1, k == 0>(a, f, xf[k & 1][1]);
        if constexpr (n + P < 96) wf[(n + P) % R][1] = whalf(n + P, 1);
        SB();
        if constexpr (!(i & 1)) { mma<k == 0>(cs[i >> 1], ones, f); SB(); }
        if constexpr ((i == 0 || i == 2) && k < 15) { xf[(k + 1) & 1][i >> 1] = xread(xa, k + 1, i >> 1); SB(); }
        if constexpr (i == 1) { mma<k == 0>(sx, ones, xt); SB(); mma<k == 0>(sq, xt, xt); SB(); }
        if constexpr (n == 26 || n == 28) { stfrag(empty, c.rws, lane * 16, OFF_O + (c.b * 4 + w) * 2048, (n - 26) / 2); SB(); }
        if constexpr (n % 12 == 11 && n < 60) { bar(); STAMP3(42 + n / 12); }
    });
    settle();
    STAMP(15);
    if (lane < 16) {
#pragma unroll
        for (int i = 0; i < 3; ++i) lds_st<float>(L_CSQ + ((w >> 1) * 96 + 32 * i + 16 * (w & 1) + lane) * 4, cs[i][0]);
    }
    asm volatile("s_waitcnt lgkmcnt(0)" ::: "memory");
    lds_st<unsigned>(L_SYNC + 16 + w * 4, 1u);
    for (unsigned n = 0; n < SPIN_CAP; ++n) { asm volatile("" ::: "memory"); if (all_lanes(lds_ld<unsigned>(L_SYNC + 16 + (lane & 3) * 4) != 0)) break; }
    asm volatile("" ::: "memory");
    STAMP3(9);
    QkvNorm nm;
    qkv_norm(nm, L_CSQ + (w >> 1) * 384, w & 1, w & 1, lane);
    QkvOut qo;
    qo.init(0, c.pair() + (w & 1), 2 * c.fp() + (w >> 1), lane);
    static_for<0, 60>([&](auto ec) AI { qo.template piece<decltype(ec)::v>(c, w & 1, lane, a, nm); });
    qo.publish(c, 0, c.pair() + (w & 1), 2 * c.fp() + (w >> 1), lane);
    STAMP3(14);
    bar();
}

DI void phaseA_help(const Ctx& c0, const void* wqkv, const void* wo, const void* wg, const void* wu, const void* wd) {
    const Ctx c = fresh(c0); const unsigned lane = lane_id();
    const unsigned hw = c.w - 4, tok0 = 32 * c.pair() + 16 * hw, rip = lane >> 3;
    const unsigned vx0 = rip * 1024 + (((lane & 7) ^ (lane >> 4)) * 16), vx1 = vx0 ^ 64;
    const unsigned vw = (8 * (rip >> 2) + (rip & 3)) * 3072 + (((lane & 7) ^ (((rip >> 1) & 3) << 1)) * 16);
    auto chunk = [&](int n) AI {
        const unsigned sl = (n & 3) * L_SLOT;
#pragma unroll
        for (int p = 0; p < 2; ++p) dma<false, 0>(c.rx, p ? vx1 : vx0, (tok0 + 8 * p) * 1024 + n * 128, sl + hw * 2048 + p * 1024);
#pragma unroll
        for (int p = 0; p < 6; ++p)
            dma<false, 0>(c.rwqkv, vw, (64 * n + 32 * (hw >> 1) + 4 * (hw & 1) + 16 * (p & 1)) * 3072 + (p >> 1) * 1024 + c.fp() * 128, sl + 8192 + (p >> 1) * 8192 + (2 * hw + (p & 1)) * 1024);
    };
    if (lane < 4) lds_st<unsigned>(L_SYNC + 16 + lane * 4, 0u);
    chunk(0); chunk(1);
    chunk(2);
    {
        const unsigned p = c.r() * 4 + hw, l = (p - 60) * 64 + lane;
        if (p < 60) warm(c.rwqkv, (lane >> 2) * 1024 + (lane & 3) * 128, (576 + 16 * p) * 1024 + c.half() * 512);
        else warm(c.rx, (l / 5) * 1024 + (l % 5) * 128 + 384, p < 100 ? (c.g() >> 1) * 524288 : SEQ * DM * 2);
    }
    STAMP3(7);
    const Unit u0 = make_unit<0>(c, wqkv, wo, wg, wu, wd), u1 = make_unit<1>(c, wqkv, wo, wg, wu, wd);
    u32x4 second[4];
    static_for<0, 6>([&](auto ic) AI {
        constexpr int n = decltype(ic)::v;
        if constexpr (n <= 1) vmwait<9>(); else vmwait<8>();
        if constexpr (n > 0 || A_START == 3) bar_lds();
        if constexpr (n <= 4) chunk(n + 3);
        if constexpr (n == 4) {
            const rsrc_t r0 = mkrsrc(u0.base, u0.bytes), r1 = mkrsrc(u1.base, u1.bytes);
#pragma unroll
            for (int p = 0; p < 4; ++p) dma<false, 0>(r0, rip * u0.pitch + (lane & 7) * 16, u0.src[p], L_EX + hw * 4096 + p * 1024);
#pragma unroll
            for (int p = 0; p < 4; ++p) second[p] = ld16<0>(r1, rip * u1.pitch + (lane & 7) * 16, u1.src[p]);
        }
    });
    const unsigned one = vconst<ONES>();
    const u32x4 ones = {one, one, one, one};
    __builtin_amdgcn_s_waitcnt(0x0f70);
    vmwait<0>();
    unit_store(c, u0, L_EX + hw * 4096, lane, ones);
#pragma unroll
    for (int p = 0; p < 4; ++p) lds_st<u32x4>(L_EX + hw * 4096 + p * 1024 + lane * 16, second[p]);
    unit_store(c, u1, L_EX + hw * 4096, lane, ones);
    vmwait<0>();
    bar_lds();
    if (lane == 0) __hip_atomic_store((gu64*)(c.ws + OFF_FW + (c.b * 4 + hw) * 8), lds_ld<u64>(L_SYNC), RLX_AGENT);
}

template <int N> DI unsigned chunk_src(const Ctx& c) {
    return (N < 2 ? OFF_WO + c.fp() * 65536 + N * 32768 : N < 9 ? OFF_WG + c.col8() * 229376 + (N - 2) * 32768 :
            N < 16 ? OFF_WD + c.fp() * 229376 + (N - 9) * 32768 : OFF_WQ + c.col8() * 98304 + (N - 16) * 32768) + (c.w - 4) * 8192;
}
template <int N, int SLEEP = 0> DI void issue_chunk(const Ctx& c, unsigned lane) {
    unsigned src = rfl(chunk_src<N>(c));
    asm volatile("s_nop 4" : "+s"(src));
    const unsigned dst = (N & 3) * L_SLOT + (c.w - 4) * 8192, vo = lane * 16;
    static_for<0, 8>([&](auto ic) AI {
        constexpr int i = decltype(ic)::v;
        dma<true, (i & 3) * 1024>(c.rws, vo, src + (i >> 2) * 4096, dst + (i >> 2) * 4096);
        if constexpr (SLEEP) __builtin_amdgcn_s_sleep(SLEEP);
    });
}
template <int N0> DI u32x4 stream_frag(unsigned lo, unsigned hi, int n) { const int sl = (N0 + (n >> 5)) & 3; return lds_ld<u32x4>((sl & 2 ? hi : lo) + (sl & 1) * L_SLOT + (n & 31) * 1024); }

template <bool SQUARE> DI void stats_to_lds(const Ctx& c, unsigned off, unsigned lane) {
    const unsigned hw = c.w - 4, tt = c.quad() + hw;
    u32x2 sp[8];
    float mu, rs;
    load_stats(c, off, tt, lane, sp);
    reduce_stats(c, off, tt, lane, sp, mu, rs);
    if (lane < 32) { lds_st<float>(L_MU + (32 * hw + lane) * 4, mu); lds_st<float>(L_RS + (32 * hw + lane) * 4, SQUARE ? rs * rs : rs); }
}

DI void phaseF(const Ctx& c0) {
    const Ctx c = fresh(c0); const unsigned lane = lane_id();
    constexpr int R = 5, P = 5, I0 = I0F;
    const unsigned w = c.w, tt = c.quad() + w, vo = lane * 16, tso = OFF_X2 + tt * 32768;
    u32x4 tok[16][2], wf[R];
    auto tload = [&](int s, u32x4& a, u32x4& b) AI { a = ldfrag(c.rws, vo, tso, 2 * s); b = ldfrag(c.rws, vo, tso, 2 * s + 1); };
    await(c.rws, (lane & 15) * 2048 + 2044, tso);
    STAMP(27);
#pragma unroll
    for (int s = 0; s < I0; ++s) tload(s, tok[s][0], tok[s][1]);
    bar();
    unsigned vh = vo + 2 * L_SLOT;
    PIN(vh);
#pragma unroll
    for (int n = 0; n < P; ++n) wf[n] = stream_frag<16>(vo, vh, n);
    QkvAcc a;
    QkvNorm nm;
    Check ck;
    static_for<0, 3>([&](auto pc) AI { ensure<decltype(pc)::v>(ck, tok[0][0], tok[0][1], [&](u32x4& x, u32x4& y) AI { tload(0, x, y); }); });
    QkvOut qo;
    qo.init(1, tt, c.col8(), lane);
    static_for<0, 96>([&](auto ic) AI {
        constexpr int n = decltype(ic)::v, k = n < 64 ? n >> 2 : (n - 64) >> 1, i = n < 64 ? 2 + (n & 3) : n & 1;
        qkv_mma<i, 0, k == 0>(a, wf[n % R], tok[k][0]);
        if constexpr (n < 60 && (n & 3) >= 1) { ensure<(n & 3) - 1>(ck, tok[k + 1][0], tok[k + 1][1], [&](u32x4& x, u32x4& y) AI { tload(k + 1, x, y); }); SB(); }
        if constexpr (n >= 67) { qo.template piece<n - 67>(c, 0, lane, a, nm); SB(); }
        qkv_mma<i, 1, k == 0>(a, wf[n % R], tok[k][1]);
        if constexpr (n + P < 96) wf[(n + P) % R] = stream_frag<16>(vo, vh, n + P);
        if constexpr (n % SPF == 0 && 2 * I0 + n / SPF < 32) { constexpr int f = 2 * I0 + n / SPF; tok[f >> 1][f & 1] = ldfrag(c.rws, vo, tso, f); }
        if constexpr (n == 47) bar();
        if constexpr (n == 61) qkv_norm(nm, L_CSQ, 0, w, lane);
        SB();
    });
    settle();
    STAMP(28);
    static_for<29, 60>([&](auto ec) AI { qo.template piece<decltype(ec)::v>(c, 0, lane, a, nm); });
    qo.publish(c, 1, tt, c.col8(), lane);
    bar();
}

#define ATT \
    "s_nop 4\n\ts_mov_b32 m0, %[kd]\n\ts_add_u32 %[kd], %[kd], 0x10000\n\tbuffer_load_dwordx4 v116, %[rs], %[sk] offen sc1 lds\n\ts_mov_b32 m0, %[kd]\n\ts_sub_u32 %[kd], %[kd], 0xf000\n\t" \
    "buffer_load_dwordx4 v116, %[rs], %[sv] offen sc1 lds\n\ts_add_u32 %[sk], %[sk], 0x1000\n\ts_add_u32 %[sv], %[sv], 0x1000\n\tbuffer_load_dwordx4 v[80:83], v116, %[rs], %[sq] offen sc1\n\t" \
    "buffer_load_dwordx4 v[84:87], v116, %[rs], %[sq1] offen sc1\n\tv_mov_b32 v48, 0\n\tv_mov_b32 v49, 0\n\tv_mov_b32 v50, 0\n\tv_mov_b32 v51, 0\n\tv_mov_b32 v52, 0\n\tv_mov_b32 v53, 0\n\t" \
    "v_mov_b32 v54, 0\n\tv_mov_b32 v55, 0\n\tv_mov_b32 v56, 0\n\tv_mov_b32 v57, 0\n\tv_mov_b32 v58, 0\n\tv_mov_b32 v59, 0\n\tv_mov_b32 v60, 0\n\tv_mov_b32 v61, 0\n\tv_mov_b32 v62, 0\n\t" \
    "v_mov_b32 v63, 0\n\tv_mov_b32 v64, 0\n\tv_mov_b32 v65, 0\n\tv_mov_b32 v66, 0\n\tv_mov_b32 v67, 0\n\tv_mov_b32 v68, 0\n\tv_mov_b32 v69, 0\n\tv_mov_b32 v70, 0\n\tv_mov_b32 v71, 0\n\t" \
    "v_mov_b32 v72, 0\n\tv_mov_b32 v73, 0\n\tv_mov_b32 v74, 0\n\tv_mov_b32 v75, 0\n\tv_mov_b32 v76, 0\n\tv_mov_b32 v77, 0\n\tv_mov_b32 v78, 0\n\tv_mov_b32 v79, 0\n\t" \
    "s_waitcnt vmcnt(0) lgkmcnt(0)\n\ts_barrier\n\tds_read_b128 v[88:91], v112\n\tds_read_b128 v[96:99], v113\n\tds_read_b128 v[100:103], v113 offset:512\n\ts_waitcnt lgkmcnt(0)\n\t" \
    "v_mfma_f32_32x32x16_bf16 v[16:31], v[88:91], v[80:83], 0\n\ts_nop 15\n\tv_exp_f32 v16, v16\n\tv_exp_f32 v17, v17\n\tv_exp_f32 v18, v18\n\tv_exp_f32 v19, v19\n\tv_exp_f32 v20, v20\n\t" \
    "v_mfma_f32_32x32x16_bf16 v[32:47], v[88:91], v[84:87], 0\n\tds_read_b128 v[92:95], v112 offset:1024\n\tds_read_b128 v[104:107], v113 offset:1024\n\t" \
    "ds_read_b128 v[108:111], v113 offset:1536\n\tv_exp_f32 v21, v21\n\tv_exp_f32 v22, v22\n\tv_exp_f32 v23, v23\n\tv_exp_f32 v24, v24\n\tv_exp_f32 v25, v25\n\tv_exp_f32 v26, v26\n\t" \
    "v_smfmac_f32_32x32x32_bf16 v[48:63], v[96:99], v[16:23], v115\n\tv_exp_f32 v27, v27\n\tv_exp_f32 v28, v28\n\tv_exp_f32 v29, v29\n\tv_exp_f32 v30, v30\n\tv_exp_f32 v31, v31\n\t" \
    "v_exp_f32 v32, v32\n\tv_exp_f32 v33, v33\n\ts_mov_b32 m0, %[kd]\n\ts_add_u32 %[kd], %[kd], 0x10000\n\tbuffer_load_dwordx4 v116, %[rs], %[sk] offen sc1 lds\n\ts_mov_b32 m0, %[kd]\n\t" \
    "s_sub_u32 %[kd], %[kd], 0xf000\n\tbuffer_load_dwordx4 v116, %[rs], %[sv] offen sc1 lds\n\ts_add_u32 %[sk], %[sk], 0x1000\n\ts_add_u32 %[sv], %[sv], 0x1000\n\t" \
    "v_smfmac_f32_32x32x32_bf16 v[48:63], v[100:103], v[24:31], v115\n\tv_exp_f32 v34, v34\n\tv_exp_f32 v35, v35\n\tv_exp_f32 v36, v36\n\ts_waitcnt lgkmcnt(0)\n\t" \
    "v_mfma_f32_32x32x16_bf16 v[16:31], v[92:95], v[80:83], 0\n\tv_exp_f32 v37, v37\n\tv_exp_f32 v38, v38\n\tv_exp_f32 v39, v39\n\tv_exp_f32 v40, v40\n\tv_exp_f32 v41, v41\n\t" \
    "v_exp_f32 v42, v42\n\tv_smfmac_f32_32x32x32_bf16 v[64:79], v[96:99], v[32:39], v115\n\tv_exp_f32 v43, v43\n\tv_exp_f32 v44, v44\n\tv_exp_f32 v45, v45\n\tv_exp_f32 v46, v46\n\t" \
    "v_exp_f32 v47, v47\n\tv_exp_f32 v16, v16\n\tv_exp_f32 v17, v17\n\ts_mov_b32 m0, %[kd]\n\ts_add_u32 %[kd], %[kd], 0x10000\n\tbuffer_load_dwordx4 v116, %[rs], %[sk] offen sc1 lds\n\t" \
    "s_mov_b32 m0, %[kd]\n\ts_sub_u32 %[kd], %[kd], 0xf000\n\tbuffer_load_dwordx4 v116, %[rs], %[sv] offen sc1 lds\n\ts_add_u32 %[sk], %[sk], 0x1000\n\ts_add_u32 %[sv], %[sv], 0x1000\n\t" \
    "v_smfmac_f32_32x32x32_bf16 v[64:79], v[100:103], v[40:47], v115\n\tv_exp_f32 v18, v18\n\tv_exp_f32 v19, v19\n\tv_exp_f32 v20, v20\n\t" \
    "v_mfma_f32_32x32x16_bf16 v[32:47], v[92:95], v[84:87], 0\n\tds_read_b128 v[88:91], v112 offset:2048\n\tds_read_b128 v[96:99], v113 offset:2048\n\t" \
    "ds_read_b128 v[100:103], v113 offset:2560\n\tv_exp_f32 v21, v21\n\tv_exp_f32 v22, v22\n\tv_exp_f32 v23, v23\n\tv_exp_f32 v24, v24\n\tv_exp_f32 v25, v25\n\tv_exp_f32 v26, v26\n\t" \
    "v_smfmac_f32_32x32x32_bf16 v[48:63], v[104:107], v[16:23], v115\n\tv_exp_f32 v27, v27\n\tv_exp_f32 v28, v28\n\tv_exp_f32 v29, v29\n\tv_exp_f32 v30, v30\n\tv_exp_f32 v31, v31\n\t" \
    "v_exp_f32 v32, v32\n\tv_exp_f32 v33, v33\n\ts_mov_b32 m0, %[kd]\n\ts_add_u32 %[kd], %[kd], 0x10000\n\tbuffer_load_dwordx4 v116, %[rs], %[sk] offen sc1 lds\n\ts_mov_b32 m0, %[kd]\n\t" \
    "s_sub_u32 %[kd], %[kd], 0xf000\n\tbuffer_load_dwordx4 v116, %[rs], %[sv] offen sc1 lds\n\ts_add_u32 %[sk], %[sk], 0x1000\n\ts_add_u32 %[sv], %[sv], 0x1000\n\t" \
    "v_smfmac_f32_32x32x32_bf16 v[48:63], v[108:111], v[24:31], v115\n\tv_exp_f32 v34, v34\n\tv_exp_f32 v35, v35\n\tv_exp_f32 v36, v36\n\ts_waitcnt lgkmcnt(0)\n\t" \
    "v_mfma_f32_32x32x16_bf16 v[16:31], v[88:91], v[80:83], 0\n\tv_exp_f32 v37, v37\n\tv_exp_f32 v38, v38\n\tv_exp_f32 v39, v39\n\tv_exp_f32 v40, v40\n\tv_exp_f32 v41, v41\n\t" \
    "v_exp_f32 v42, v42\n\tv_smfmac_f32_32x32x32_bf16 v[64:79], v[104:107], v[32:39], v115\n\tv_exp_f32 v43, v43\n\tv_exp_f32 v44, v44\n\tv_exp_f32 v45, v45\n\tv_exp_f32 v46, v46\n\t" \
    "v_exp_f32 v47, v47\n\tv_exp_f32 v16, v16\n\tv_exp_f32 v17, v17\n\ts_mov_b32 m0, %[kd]\n\ts_add_u32 %[kd], %[kd], 0x10000\n\tbuffer_load_dwordx4 v116, %[rs], %[sk] offen sc1 lds\n\t" \
    "s_mov_b32 m0, %[kd]\n\ts_sub_u32 %[kd], %[kd], 0xf000\n\tbuffer_load_dwordx4 v116, %[rs], %[sv] offen sc1 lds\n\ts_add_u32 %[sk], %[sk], 0x1000\n\ts_add_u32 %[sv], %[sv], 0x1000\n\t" \
    "v_smfmac_f32_32x32x32_bf16 v[64:79], v[108:111], v[40:47], v115\n\tv_exp_f32 v18, v18\n\tv_exp_f32 v19, v19\n\tv_exp_f32 v20, v20\n\t" \
    "v_mfma_f32_32x32x16_bf16 v[32:47], v[88:91], v[84:87], 0\n\tds_read_b128 v[92:95], v112 offset:3072\n\tds_read_b128 v[104:107], v113 offset:3072\n\t" \
    "ds_read_b128 v[108:111], v113 offset:3584\n\tv_exp_f32 v21, v21\n\tv_exp_f32 v22, v22\n\tv_exp_f32 v23, v23\n\tv_exp_f32 v24, v24\n\tv_exp_f32 v25, v25\n\tv_exp_f32 v26, v26\n\t" \
    "v_smfmac_f32_32x32x32_bf16 v[48:63], v[96:99], v[16:23], v115\n\tv_exp_f32 v27, v27\n\tv_exp_f32 v28, v28\n\tv_exp_f32 v29, v29\n\tv_exp_f32 v30, v30\n\tv_exp_f32 v31, v31\n\t" \
    "v_exp_f32 v32, v32\n\tv_exp_f32 v33, v33\n\ts_mov_b32 m0, %[kd]\n\ts_add_u32 %[kd], %[kd], 0x10000\n\tbuffer_load_dwordx4 v116, %[rs], %[sk] offen sc1 lds\n\ts_mov_b32 m0, %[kd]\n\t" \
    "s_sub_u32 %[kd], %[kd], 0xf000\n\tbuffer_load_dwordx4 v116, %[rs], %[sv] offen sc1 lds\n\ts_add_u32 %[sk], %[sk], 0x1000\n\ts_add_u32 %[sv], %[sv], 0x1000\n\t" \
    "v_smfmac_f32_32x32x32_bf16 v[48:63], v[100:103], v[24:31], v115\n\tv_exp_f32 v34, v34\n\tv_exp_f32 v35, v35\n\tv_exp_f32 v36, v36\n\ts_waitcnt lgkmcnt(0)\n\t" \
    "v_mfma_f32_32x32x16_bf16 v[16:31], v[92:95], v[80:83], 0\n\tv_exp_f32 v37, v37\n\tv_exp_f32 v38, v38\n\tv_exp_f32 v39, v39\n\tv_exp_f32 v40, v40\n\tv_exp_f32 v41, v41\n\t" \
    "v_exp_f32 v42, v42\n\tv_smfmac_f32_32x32x32_bf16 v[64:79], v[96:99], v[32:39], v115\n\tv_exp_f32 v43, v43\n\tv_exp_f32 v44, v44\n\tv_exp_f32 v45, v45\n\tv_exp_f32 v46, v46\n\t" \
    "v_exp_f32 v47, v47\n\tv_exp_f32 v16, v16\n\tv_exp_f32 v17, v17\n\ts_mov_b32 m0, %[kd]\n\ts_add_u32 %[kd], %[kd], 0x10000\n\tbuffer_load_dwordx4 v116, %[rs], %[sk] offen sc1 lds\n\t" \
    "s_mov_b32 m0, %[kd]\n\ts_sub_u32 %[kd], %[kd], 0xf000\n\tbuffer_load_dwordx4 v116, %[rs], %[sv] offen sc1 lds\n\ts_add_u32 %[sk], %[sk], 0x1000\n\ts_add_u32 %[sv], %[sv], 0x1000\n\t" \
    "v_smfmac_f32_32x32x32_bf16 v[64:79], v[100:103], v[40:47], v115\n\ts_waitcnt vmcnt(10)\n\ts_barrier\n\tv_exp_f32 v18, v18\n\tv_exp_f32 v19, v19\n\tv_exp_f32 v20, v20\n\t" \
    "v_mfma_f32_32x32x16_bf16 v[32:47], v[92:95], v[84:87], 0\n\tv_add_u32 v113, v113, v114\n\tds_read_b128 v[88:91], v112 offset:4096\n\tds_read_b128 v[96:99], v113 offset:0\n\t" \
    "ds_read_b128 v[100:103], v113 offset:512\n\tv_exp_f32 v21, v21\n\tv_exp_f32 v22, v22\n\tv_exp_f32 v23, v23\n\tv_exp_f32 v24, v24\n\tv_exp_f32 v25, v25\n\tv_exp_f32 v26, v26\n\t" \
    "v_smfmac_f32_32x32x32_bf16 v[48:63], v[104:107], v[16:23], v115\n\tv_exp_f32 v27, v27\n\tv_exp_f32 v28, v28\n\tv_exp_f32 v29, v29\n\tv_exp_f32 v30, v30\n\tv_exp_f32 v31, v31\n\t" \
    "v_exp_f32 v32, v32\n\tv_exp_f32 v33, v33\n\ts_mov_b32 m0, %[kd]\n\ts_add_u32 %[kd], %[kd], 0x10000\n\tbuffer_load_dwordx4 v116, %[rs], %[sk] offen sc1 lds\n\ts_mov_b32 m0, %[kd]\n\t" \
    "s_sub_u32 %[kd], %[kd], 0xf000\n\tbuffer_load_dwordx4 v116, %[rs], %[sv] offen sc1 lds\n\ts_add_u32 %[sk], %[sk], 0x1000\n\ts_add_u32 %[sv], %[sv], 0x1000\n\t" \
    "v_smfmac_f32_32x32x32_bf16 v[48:63], v[108:111], v[24:31], v115\n\tv_exp_f32 v34, v34\n\tv_exp_f32 v35, v35\n\tv_exp_f32 v36, v36\n\ts_waitcnt lgkmcnt(0)\n\t" \
    "v_mfma_f32_32x32x16_bf16 v[16:31], v[88:91], v[80:83], 0\n\tv_exp_f32 v37, v37\n\tv_exp_f32 v38, v38\n\tv_exp_f32 v39, v39\n\tv_exp_f32 v40, v40\n\tv_exp_f32 v41, v41\n\t" \
    "v_exp_f32 v42, v42\n\tv_smfmac_f32_32x32x32_bf16 v[64:79], v[104:107], v[32:39], v115\n\tv_exp_f32 v43, v43\n\tv_exp_f32 v44, v44\n\tv_exp_f32 v45, v45\n\tv_exp_f32 v46, v46\n\t" \
    "v_exp_f32 v47, v47\n\tv_exp_f32 v16, v16\n\tv_exp_f32 v17, v17\n\tv_smfmac_f32_32x32x32_bf16 v[64:79], v[108:111], v[40:47], v115\n\tv_exp_f32 v18, v18\n\tv_exp_f32 v19, v19\n\t" \
    "v_exp_f32 v20, v20\n\tv_mfma_f32_32x32x16_bf16 v[32:47], v[88:91], v[84:87], 0\n\tds_read_b128 v[92:95], v112 offset:5120\n\tds_read_b128 v[104:107], v113 offset:1024\n\t" \
    "ds_read_b128 v[108:111], v113 offset:1536\n\tv_exp_f32 v21, v21\n\tv_exp_f32 v22, v22\n\tv_exp_f32 v23, v23\n\tv_exp_f32 v24, v24\n\tv_exp_f32 v25, v25\n\tv_exp_f32 v26, v26\n\t" \
    "v_smfmac_f32_32x32x32_bf16 v[48:63], v[96:99], v[16:23], v115\n\tv_exp_f32 v27, v27\n\tv_exp_f32 v28, v28\n\tv_exp_f32 v29, v29\n\tv_exp_f32 v30, v30\n\tv_exp_f32 v31, v31\n\t" \
    "v_exp_f32 v32, v32\n\tv_exp_f32 v33, v33\n\tv_smfmac_f32_32x32x32_bf16 v[48:63], v[100:103], v[24:31], v115\n\tv_exp_f32 v34, v34\n\tv_exp_f32 v35, v35\n\tv_exp_f32 v36, v36\n\t" \
    "s_waitcnt lgkmcnt(0)\n\tv_mfma_f32_32x32x16_bf16 v[16:31], v[92:95], v[80:83], 0\n\tv_exp_f32 v37, v37\n\tv_exp_f32 v38, v38\n\tv_exp_f32 v39, v39\n\tv_exp_f32 v40, v40\n\t" \
    "v_exp_f32 v41, v41\n\tv_exp_f32 v42, v42\n\tv_smfmac_f32_32x32x32_bf16 v[64:79], v[96:99], v[32:39], v115\n\tv_exp_f32 v43, v43\n\tv_exp_f32 v44, v44\n\tv_exp_f32 v45, v45\n\t" \
    "v_exp_f32 v46, v46\n\tv_exp_f32 v47, v47\n\tv_exp_f32 v16, v16\n\tv_exp_f32 v17, v17\n\tv_smfmac_f32_32x32x32_bf16 v[64:79], v[100:103], v[40:47], v115\n\tv_exp_f32 v18, v18\n\t" \
    "v_exp_f32 v19, v19\n\tv_exp_f32 v20, v20\n\tv_mfma_f32_32x32x16_bf16 v[32:47], v[92:95], v[84:87], 0\n\tds_read_b128 v[88:91], v112 offset:6144\n\tds_read_b128 v[96:99], v113 offset:2048\n\t" \
    "ds_read_b128 v[100:103], v113 offset:2560\n\tv_exp_f32 v21, v21\n\tv_exp_f32 v22, v22\n\tv_exp_f32 v23, v23\n\tv_exp_f32 v24, v24\n\tv_exp_f32 v25, v25\n\tv_exp_f32 v26, v26\n\t" \
    "v_smfmac_f32_32x32x32_bf16 v[48:63], v[104:107], v[16:23], v115\n\tv_exp_f32 v27, v27\n\tv_exp_f32 v28, v28\n\tv_exp_f32 v29, v29\n\tv_exp_f32 v30, v30\n\tv_exp_f32 v31, v31\n\t" \
    "v_exp_f32 v32, v32\n\tv_exp_f32 v33, v33\n\tv_smfmac_f32_32x32x32_bf16 v[48:63], v[108:111], v[24:31], v115\n\tv_exp_f32 v34, v34\n\tv_exp_f32 v35, v35\n\tv_exp_f32 v36, v36\n\t" \
    "s_waitcnt lgkmcnt(0)\n\tv_mfma_f32_32x32x16_bf16 v[16:31], v[88:91], v[80:83], 0\n\tv_exp_f32 v37, v37\n\tv_exp_f32 v38, v38\n\tv_exp_f32 v39, v39\n\tv_exp_f32 v40, v40\n\t" \
    "v_exp_f32 v41, v41\n\tv_exp_f32 v42, v42\n\tv_smfmac_f32_32x32x32_bf16 v[64:79], v[104:107], v[32:39], v115\n\tv_exp_f32 v43, v43\n\tv_exp_f32 v44, v44\n\tv_exp_f32 v45, v45\n\t" \
    "v_exp_f32 v46, v46\n\tv_exp_f32 v47, v47\n\tv_exp_f32 v16, v16\n\tv_exp_f32 v17, v17\n\tbuffer_load_dwordx4 v[120:123], v116, %[rs], %[sfw] offen sc1\n\t" \
    "v_smfmac_f32_32x32x32_bf16 v[64:79], v[108:111], v[40:47], v115\n\tv_exp_f32 v18, v18\n\tv_exp_f32 v19, v19\n\tv_exp_f32 v20, v20\n\t" \
    "v_mfma_f32_32x32x16_bf16 v[32:47], v[88:91], v[84:87], 0\n\tds_read_b128 v[92:95], v112 offset:7168\n\tds_read_b128 v[104:107], v113 offset:3072\n\t" \
    "ds_read_b128 v[108:111], v113 offset:3584\n\tv_exp_f32 v21, v21\n\tv_exp_f32 v22, v22\n\tv_exp_f32 v23, v23\n\tv_exp_f32 v24, v24\n\tv_exp_f32 v25, v25\n\tv_exp_f32 v26, v26\n\t" \
    "v_smfmac_f32_32x32x32_bf16 v[48:63], v[96:99], v[16:23], v115\n\tv_exp_f32 v27, v27\n\tv_exp_f32 v28, v28\n\tv_exp_f32 v29, v29\n\tv_exp_f32 v30, v30\n\tv_exp_f32 v31, v31\n\t" \
    "v_exp_f32 v32, v32\n\tv_exp_f32 v33, v33\n\tv_smfmac_f32_32x32x32_bf16 v[48:63], v[100:103], v[24:31], v115\n\tv_exp_f32 v34, v34\n\tv_exp_f32 v35, v35\n\tv_exp_f32 v36, v36\n\t" \
    "s_waitcnt lgkmcnt(0)\n\tv_mfma_f32_32x32x16_bf16 v[16:31], v[92:95], v[80:83], 0\n\tv_exp_f32 v37, v37\n\tv_exp_f32 v38, v38\n\tv_exp_f32 v39, v39\n\tv_exp_f32 v40, v40\n\t" \
    "v_exp_f32 v41, v41\n\tv_exp_f32 v42, v42\n\tv_smfmac_f32_32x32x32_bf16 v[64:79], v[96:99], v[32:39], v115\n\tv_exp_f32 v43, v43\n\tv_exp_f32 v44, v44\n\tv_exp_f32 v45, v45\n\t" \
    "v_exp_f32 v46, v46\n\tv_exp_f32 v47, v47\n\tv_exp_f32 v16, v16\n\tv_exp_f32 v17, v17\n\tv_smfmac_f32_32x32x32_bf16 v[64:79], v[100:103], v[40:47], v115\n\ts_mov_b32 %[sp], 0x40000\n\t2:\n\t" \
    "s_waitcnt vmcnt(0)\n\tv_cmp_eq_u64 vcc, %[tag], v[120:121]\n\tv_cmp_eq_u64 %[m2], %[tag], v[122:123]\n\ts_nop 1\n\ts_and_b64 vcc, vcc, %[m2]\n\ts_cmp_eq_u64 vcc, -1\n\ts_cbranch_scc1 3f\n\t" \
    "buffer_load_dwordx4 v[120:123], v116, %[rs], %[sfw] offen sc1\n\ts_sub_u32 %[sp], %[sp], 1\n\ts_cmp_lg_u32 %[sp], 0\n\ts_cbranch_scc1 2b\n\t3:\n\ts_barrier\n\tv_exp_f32 v18, v18\n\t" \
    "v_exp_f32 v19, v19\n\tv_exp_f32 v20, v20\n\tv_mfma_f32_32x32x16_bf16 v[32:47], v[92:95], v[84:87], 0\n\tv_add_u32 v113, v113, v114\n\tds_read_b128 v[88:91], v112 offset:8192\n\t" \
    "ds_read_b128 v[96:99], v113 offset:0\n\tds_read_b128 v[100:103], v113 offset:512\n\tv_exp_f32 v21, v21\n\tv_exp_f32 v22, v22\n\tv_exp_f32 v23, v23\n\tv_exp_f32 v24, v24\n\t" \
    "v_exp_f32 v25, v25\n\tv_exp_f32 v26, v26\n\tv_smfmac_f32_32x32x32_bf16 v[48:63], v[104:107], v[16:23], v115\n\tv_exp_f32 v27, v27\n\tv_exp_f32 v28, v28\n\tv_exp_f32 v29, v29\n\t" \
    "v_exp_f32 v30, v30\n\tv_exp_f32 v31, v31\n\tv_exp_f32 v32, v32\n\tv_exp_f32 v33, v33\n\ts_mov_b32 m0, %[wd]\n\ts_nop 0\n\tbuffer_load_dword v117, %[rs], %[w0] offen sc1 lds\n\t" \
    "v_smfmac_f32_32x32x32_bf16 v[48:63], v[108:111], v[24:31], v115\n\tv_exp_f32 v34, v34\n\tv_exp_f32 v35, v35\n\tv_exp_f32 v36, v36\n\ts_waitcnt lgkmcnt(0)\n\t" \
    "v_mfma_f32_32x32x16_bf16 v[16:31], v[88:91], v[80:83], 0\n\tv_exp_f32 v37, v37\n\tv_exp_f32 v38, v38\n\tv_exp_f32 v39, v39\n\tv_exp_f32 v40, v40\n\tv_exp_f32 v41, v41\n\t" \
    "v_exp_f32 v42, v42\n\tv_smfmac_f32_32x32x32_bf16 v[64:79], v[104:107], v[32:39], v115\n\tv_exp_f32 v43, v43\n\tv_exp_f32 v44, v44\n\tv_exp_f32 v45, v45\n\tv_exp_f32 v46, v46\n\t" \
    "v_exp_f32 v47, v47\n\tv_exp_f32 v16, v16\n\tv_exp_f32 v17, v17\n\ts_mov_b32 m0, %[wd]\n\ts_nop 0\n\tbuffer_load_dword v117, %[rs], %[w1] offen sc1 lds\n\t" \
    "v_smfmac_f32_32x32x32_bf16 v[64:79], v[108:111], v[40:47], v115\n\tv_add_u32 v112, 0x2000, v112\n\t1:\n\tv_exp_f32 v18, v18\n\tv_exp_f32 v19, v19\n\tv_exp_f32 v20, v20\n\t" \
    "v_mfma_f32_32x32x16_bf16 v[32:47], v[88:91], v[84:87], 0\n\tds_read_b128 v[92:95], v112 offset:1024\n\tds_read_b128 v[104:107], v113 offset:1024\n\t" \
    "ds_read_b128 v[108:111], v113 offset:1536\n\tv_exp_f32 v21, v21\n\tv_exp_f32 v22, v22\n\tv_exp_f32 v23, v23\n\tv_exp_f32 v24, v24\n\tv_exp_f32 v25, v25\n\tv_exp_f32 v26, v26\n\t" \
    "v_smfmac_f32_32x32x32_bf16 v[48:63], v[96:99], v[16:23], v115\n\tv_exp_f32 v27, v27\n\tv_exp_f32 v28, v28\n\tv_exp_f32 v29, v29\n\tv_exp_f32 v30, v30\n\tv_exp_f32 v31, v31\n\t" \
    "v_exp_f32 v32, v32\n\tv_exp_f32 v33, v33\n\tv_smfmac_f32_32x32x32_bf16 v[48:63], v[100:103], v[24:31], v115\n\tv_exp_f32 v34, v34\n\tv_exp_f32 v35, v35\n\tv_exp_f32 v36, v36\n\t" \
    "s_waitcnt lgkmcnt(0)\n\tv_mfma_f32_32x32x16_bf16 v[16:31], v[92:95], v[80:83], 0\n\tv_exp_f32 v37, v37\n\tv_exp_f32 v38, v38\n\tv_exp_f32 v39, v39\n\tv_exp_f32 v40, v40\n\t" \
    "v_exp_f32 v41, v41\n\tv_exp_f32 v42, v42\n\tv_smfmac_f32_32x32x32_bf16 v[64:79], v[96:99], v[32:39], v115\n\tv_exp_f32 v43, v43\n\tv_exp_f32 v44, v44\n\tv_exp_f32 v45, v45\n\t" \
    "v_exp_f32 v46, v46\n\tv_exp_f32 v47, v47\n\tv_exp_f32 v16, v16\n\tv_exp_f32 v17, v17\n\tv_smfmac_f32_32x32x32_bf16 v[64:79], v[100:103], v[40:47], v115\n\tv_exp_f32 v18, v18\n\t" \
    "v_exp_f32 v19, v19\n\tv_exp_f32 v20, v20\n\tv_mfma_f32_32x32x16_bf16 v[32:47], v[92:95], v[84:87], 0\n\tds_read_b128 v[88:91], v112 offset:2048\n\tds_read_b128 v[96:99], v113 offset:2048\n\t" \
    "ds_read_b128 v[100:103], v113 offset:2560\n\tv_exp_f32 v21, v21\n\tv_exp_f32 v22, v22\n\tv_exp_f32 v23, v23\n\tv_exp_f32 v24, v24\n\tv_exp_f32 v25, v25\n\tv_exp_f32 v26, v26\n\t" \
    "v_smfmac_f32_32x32x32_bf16 v[48:63], v[104:107], v[16:23], v115\n\tv_exp_f32 v27, v27\n\tv_exp_f32 v28, v28\n\tv_exp_f32 v29, v29\n\tv_exp_f32 v30, v30\n\tv_exp_f32 v31, v31\n\t" \
    "v_exp_f32 v32, v32\n\tv_exp_f32 v33, v33\n\tv_smfmac_f32_32x32x32_bf16 v[48:63], v[108:111], v[24:31], v115\n\tv_exp_f32 v34, v34\n\tv_exp_f32 v35, v35\n\tv_exp_f32 v36, v36\n\t" \
    "s_waitcnt lgkmcnt(0)\n\tv_mfma_f32_32x32x16_bf16 v[16:31], v[88:91], v[80:83], 0\n\tv_exp_f32 v37, v37\n\tv_exp_f32 v38, v38\n\tv_exp_f32 v39, v39\n\tv_exp_f32 v40, v40\n\t" \
    "v_exp_f32 v41, v41\n\tv_exp_f32 v42, v42\n\tv_smfmac_f32_32x32x32_bf16 v[64:79], v[104:107], v[32:39], v115\n\tv_exp_f32 v43, v43\n\tv_exp_f32 v44, v44\n\tv_exp_f32 v45, v45\n\t" \
    "v_exp_f32 v46, v46\n\tv_exp_f32 v47, v47\n\tv_exp_f32 v16, v16\n\tv_exp_f32 v17, v17\n\tv_smfmac_f32_32x32x32_bf16 v[64:79], v[108:111], v[40:47], v115\n\tv_exp_f32 v18, v18\n\t" \
    "v_exp_f32 v19, v19\n\tv_exp_f32 v20, v20\n\tv_mfma_f32_32x32x16_bf16 v[32:47], v[88:91], v[84:87], 0\n\tds_read_b128 v[92:95], v112 offset:3072\n\t" \
    "ds_read_b128 v[104:107], v113 offset:3072\n\tds_read_b128 v[108:111], v113 offset:3584\n\tv_exp_f32 v21, v21\n\tv_exp_f32 v22, v22\n\tv_exp_f32 v23, v23\n\tv_exp_f32 v24, v24\n\t" \
    "v_exp_f32 v25, v25\n\tv_exp_f32 v26, v26\n\tv_smfmac_f32_32x32x32_bf16 v[48:63], v[96:99], v[16:23], v115\n\tv_exp_f32 v27, v27\n\tv_exp_f32 v28, v28\n\tv_exp_f32 v29, v29\n\t" \
    "v_exp_f32 v30, v30\n\tv_exp_f32 v31, v31\n\tv_exp_f32 v32, v32\n\tv_exp_f32 v33, v33\n\tv_smfmac_f32_32x32x32_bf16 v[48:63], v[100:103], v[24:31], v115\n\tv_exp_f32 v34, v34\n\t" \
    "v_exp_f32 v35, v35\n\tv_exp_f32 v36, v36\n\ts_waitcnt lgkmcnt(0)\n\tv_mfma_f32_32x32x16_bf16 v[16:31], v[92:95], v[80:83], 0\n\tv_exp_f32 v37, v37\n\tv_exp_f32 v38, v38\n\t" \
    "v_exp_f32 v39, v39\n\tv_exp_f32 v40, v40\n\tv_exp_f32 v41, v41\n\tv_exp_f32 v42, v42\n\tv_smfmac_f32_32x32x32_bf16 v[64:79], v[96:99], v[32:39], v115\n\tv_exp_f32 v43, v43\n\t" \
    "v_exp_f32 v44, v44\n\tv_exp_f32 v45, v45\n\tv_exp_f32 v46, v46\n\tv_exp_f32 v47, v47\n\tv_exp_f32 v16, v16\n\tv_exp_f32 v17, v17\n\t" \
    "v_smfmac_f32_32x32x32_bf16 v[64:79], v[100:103], v[40:47], v115\n\tv_exp_f32 v18, v18\n\tv_exp_f32 v19, v19\n\tv_exp_f32 v20, v20\n\t" \
    "v_mfma_f32_32x32x16_bf16 v[32:47], v[92:95], v[84:87], 0\n\tv_add_u32 v113, v113, v114\n\tds_read_b128 v[88:91], v112 offset:4096\n\tds_read_b128 v[96:99], v113 offset:0\n\t" \
    "ds_read_b128 v[100:103], v113 offset:512\n\tv_exp_f32 v21, v21\n\tv_exp_f32 v22, v22\n\tv_exp_f32 v23, v23\n\tv_exp_f32 v24, v24\n\tv_exp_f32 v25, v25\n\tv_exp_f32 v26, v26\n\t" \
    "v_smfmac_f32_32x32x32_bf16 v[48:63], v[104:107], v[16:23], v115\n\tv_exp_f32 v27, v27\n\tv_exp_f32 v28, v28\n\tv_exp_f32 v29, v29\n\tv_exp_f32 v30, v30\n\tv_exp_f32 v31, v31\n\t" \
    "v_exp_f32 v32, v32\n\tv_exp_f32 v33, v33\n\tv_smfmac_f32_32x32x32_bf16 v[48:63], v[108:111], v[24:31], v115\n\tv_exp_f32 v34, v34\n\tv_exp_f32 v35, v35\n\tv_exp_f32 v36, v36\n\t" \
    "s_waitcnt lgkmcnt(0)\n\tv_mfma_f32_32x32x16_bf16 v[16:31], v[88:91], v[80:83], 0\n\tv_exp_f32 v37, v37\n\tv_exp_f32 v38, v38\n\tv_exp_f32 v39, v39\n\tv_exp_f32 v40, v40\n\t" \
    "v_exp_f32 v41, v41\n\tv_exp_f32 v42, v42\n\tv_smfmac_f32_32x32x32_bf16 v[64:79], v[104:107], v[32:39], v115\n\tv_exp_f32 v43, v43\n\tv_exp_f32 v44, v44\n\tv_exp_f32 v45, v45\n\t" \
    "v_exp_f32 v46, v46\n\tv_exp_f32 v47, v47\n\tv_exp_f32 v16, v16\n\tv_exp_f32 v17, v17\n\tv_smfmac_f32_32x32x32_bf16 v[64:79], v[108:111], v[40:47], v115\n\tv_exp_f32 v18, v18\n\t" \
    "v_exp_f32 v19, v19\n\tv_exp_f32 v20, v20\n\tv_mfma_f32_32x32x16_bf16 v[32:47], v[88:91], v[84:87], 0\n\tds_read_b128 v[92:95], v112 offset:5120\n\t" \
    "ds_read_b128 v[104:107], v113 offset:1024\n\tds_read_b128 v[108:111], v113 offset:1536\n\tv_exp_f32 v21, v21\n\tv_exp_f32 v22, v22\n\tv_exp_f32 v23, v23\n\tv_exp_f32 v24, v24\n\t" \
    "v_exp_f32 v25, v25\n\tv_exp_f32 v26, v26\n\tv_smfmac_f32_32x32x32_bf16 v[48:63], v[96:99], v[16:23], v115\n\tv_exp_f32 v27, v27\n\tv_exp_f32 v28, v28\n\tv_exp_f32 v29, v29\n\t" \
    "v_exp_f32 v30, v30\n\tv_exp_f32 v31, v31\n\tv_exp_f32 v32, v32\n\tv_exp_f32 v33, v33\n\tv_smfmac_f32_32x32x32_bf16 v[48:63], v[100:103], v[24:31], v115\n\tv_exp_f32 v34, v34\n\t" \
    "v_exp_f32 v35, v35\n\tv_exp_f32 v36, v36\n\ts_waitcnt lgkmcnt(0)\n\tv_mfma_f32_32x32x16_bf16 v[16:31], v[92:95], v[80:83], 0\n\tv_exp_f32 v37, v37\n\tv_exp_f32 v38, v38\n\t" \
    "v_exp_f32 v39, v39\n\tv_exp_f32 v40, v40\n\tv_exp_f32 v41, v41\n\tv_exp_f32 v42, v42\n\tv_smfmac_f32_32x32x32_bf16 v[64:79], v[96:99], v[32:39], v115\n\tv_exp_f32 v43, v43\n\t" \
    "v_exp_f32 v44, v44\n\tv_exp_f32 v45, v45\n\tv_exp_f32 v46, v46\n\tv_exp_f32 v47, v47\n\tv_exp_f32 v16, v16\n\tv_exp_f32 v17, v17\n\t" \
    "v_smfmac_f32_32x32x32_bf16 v[64:79], v[100:103], v[40:47], v115\n\tv_exp_f32 v18, v18\n\tv_exp_f32 v19, v19\n\tv_exp_f32 v20, v20\n\t" \
    "v_mfma_f32_32x32x16_bf16 v[32:47], v[92:95], v[84:87], 0\n\tds_read_b128 v[88:91], v112 offset:6144\n\tds_read_b128 v[96:99], v113 offset:2048\n\t" \
    "ds_read_b128 v[100:103], v113 offset:2560\n\tv_exp_f32 v21, v21\n\tv_exp_f32 v22, v22\n\tv_exp_f32 v23, v23\n\tv_exp_f32 v24, v24\n\tv_exp_f32 v25, v25\n\tv_exp_f32 v26, v26\n\t" \
    "v_smfmac_f32_32x32x32_bf16 v[48:63], v[104:107], v[16:23], v115\n\tv_exp_f32 v27, v27\n\tv_exp_f32 v28, v28\n\tv_exp_f32 v29, v29\n\tv_exp_f32 v30, v30\n\tv_exp_f32 v31, v31\n\t" \
    "v_exp_f32 v32, v32\n\tv_exp_f32 v33, v33\n\tv_smfmac_f32_32x32x32_bf16 v[48:63], v[108:111], v[24:31], v115\n\tv_exp_f32 v34, v34\n\tv_exp_f32 v35, v35\n\tv_exp_f32 v36, v36\n\t" \
    "s_waitcnt lgkmcnt(0)\n\tv_mfma_f32_32x32x16_bf16 v[16:31], v[88:91], v[80:83], 0\n\tv_exp_f32 v37, v37\n\tv_exp_f32 v38, v38\n\tv_exp_f32 v39, v39\n\tv_exp_f32 v40, v40\n\t" \
    "v_exp_f32 v41, v41\n\tv_exp_f32 v42, v42\n\tv_smfmac_f32_32x32x32_bf16 v[64:79], v[104:107], v[32:39], v115\n\tv_exp_f32 v43, v43\n\tv_exp_f32 v44, v44\n\tv_exp_f32 v45, v45\n\t" \
    "v_exp_f32 v46, v46\n\tv_exp_f32 v47, v47\n\tv_exp_f32 v16, v16\n\tv_exp_f32 v17, v17\n\tv_smfmac_f32_32x32x32_bf16 v[64:79], v[108:111], v[40:47], v115\n\tv_exp_f32 v18, v18\n\t" \
    "v_exp_f32 v19, v19\n\tv_exp_f32 v20, v20\n\tv_mfma_f32_32x32x16_bf16 v[32:47], v[88:91], v[84:87], 0\n\tds_read_b128 v[92:95], v112 offset:7168\n\t" \
    "ds_read_b128 v[104:107], v113 offset:3072\n\tds_read_b128 v[108:111], v113 offset:3584\n\tv_exp_f32 v21, v21\n\tv_exp_f32 v22, v22\n\tv_exp_f32 v23, v23\n\tv_exp_f32 v24, v24\n\t" \
    "v_exp_f32 v25, v25\n\tv_exp_f32 v26, v26\n\tv_smfmac_f32_32x32x32_bf16 v[48:63], v[96:99], v[16:23], v115\n\tv_exp_f32 v27, v27\n\tv_exp_f32 v28, v28\n\tv_exp_f32 v29, v29\n\t" \
    "v_exp_f32 v30, v30\n\tv_exp_f32 v31, v31\n\tv_exp_f32 v32, v32\n\tv_exp_f32 v33, v33\n\tv_smfmac_f32_32x32x32_bf16 v[48:63], v[100:103], v[24:31], v115\n\tv_exp_f32 v34, v34\n\t" \
    "v_exp_f32 v35, v35\n\tv_exp_f32 v36, v36\n\ts_waitcnt lgkmcnt(0)\n\tv_mfma_f32_32x32x16_bf16 v[16:31], v[92:95], v[80:83], 0\n\tv_exp_f32 v37, v37\n\tv_exp_f32 v38, v38\n\t" \
    "v_exp_f32 v39, v39\n\tv_exp_f32 v40, v40\n\tv_exp_f32 v41, v41\n\tv_exp_f32 v42, v42\n\tv_smfmac_f32_32x32x32_bf16 v[64:79], v[96:99], v[32:39], v115\n\tv_exp_f32 v43, v43\n\t" \
    "v_exp_f32 v44, v44\n\tv_exp_f32 v45, v45\n\tv_exp_f32 v46, v46\n\tv_exp_f32 v47, v47\n\tv_exp_f32 v16, v16\n\tv_exp_f32 v17, v17\n\t" \
    "v_smfmac_f32_32x32x32_bf16 v[64:79], v[100:103], v[40:47], v115\n\tv_exp_f32 v18, v18\n\tv_exp_f32 v19, v19\n\tv_exp_f32 v20, v20\n\t" \
    "v_mfma_f32_32x32x16_bf16 v[32:47], v[92:95], v[84:87], 0\n\tv_add_u32 v113, v113, v114\n\tds_read_b128 v[88:91], v112 offset:8192\n\tds_read_b128 v[96:99], v113 offset:0\n\t" \
    "ds_read_b128 v[100:103], v113 offset:512\n\tv_exp_f32 v21, v21\n\tv_exp_f32 v22, v22\n\tv_exp_f32 v23, v23\n\tv_exp_f32 v24, v24\n\tv_exp_f32 v25, v25\n\tv_exp_f32 v26, v26\n\t" \
    "v_smfmac_f32_32x32x32_bf16 v[48:63], v[104:107], v[16:23], v115\n\tv_exp_f32 v27, v27\n\tv_exp_f32 v28, v28\n\tv_exp_f32 v29, v29\n\tv_exp_f32 v30, v30\n\tv_exp_f32 v31, v31\n\t" \
    "v_exp_f32 v32, v32\n\tv_exp_f32 v33, v33\n\tv_smfmac_f32_32x32x32_bf16 v[48:63], v[108:111], v[24:31], v115\n\tv_exp_f32 v34, v34\n\tv_exp_f32 v35, v35\n\tv_exp_f32 v36, v36\n\t" \
    "s_waitcnt lgkmcnt(0)\n\tv_mfma_f32_32x32x16_bf16 v[16:31], v[88:91], v[80:83], 0\n\tv_exp_f32 v37, v37\n\tv_exp_f32 v38, v38\n\tv_exp_f32 v39, v39\n\tv_exp_f32 v40, v40\n\t" \
    "v_exp_f32 v41, v41\n\tv_exp_f32 v42, v42\n\tv_smfmac_f32_32x32x32_bf16 v[64:79], v[104:107], v[32:39], v115\n\tv_exp_f32 v43, v43\n\tv_exp_f32 v44, v44\n\tv_exp_f32 v45, v45\n\t" \
    "v_exp_f32 v46, v46\n\tv_exp_f32 v47, v47\n\tv_exp_f32 v16, v16\n\tv_exp_f32 v17, v17\n\ts_sub_u32 %[cnt], %[cnt], 1\n\tv_smfmac_f32_32x32x32_bf16 v[64:79], v[108:111], v[40:47], v115\n\t" \
    "v_add_u32 v112, 0x2000, v112\n\ts_cmp_lg_u32 %[cnt], 0\n\ts_cbranch_scc1 1b\n\ts_nop 15\n\t"

DI void attention_true_max(unsigned lane, u32x4 qf, unsigned va, unsigned vstep, int idx, f32x16& o) {
    float m = -__builtin_inff();
#pragma unroll 1
    for (unsigned i = 0; i < 64; ++i) {
        const f32x16 s = mfma32(lds_ld<u32x4>(i * 1024 + lane * 16), qf, f32x16{});
#pragma unroll
        for (int e = 0; e < 16; ++e) m = __builtin_fmaxf(m, s[e]);
    }
    m = -swap32_max(m);
    f32x16 ci;
#pragma unroll
    for (int e = 0; e < 16; ++e) ci[e] = m;
    o = f32x16{};
#pragma unroll 1
    for (unsigned i = 0; i < 64; ++i) {
        f32x16 s = mfma32(lds_ld<u32x4>(i * 1024 + lane * 16), qf, ci);
#pragma unroll
        for (int e = 0; e < 16; ++e) s[e] = __builtin_amdgcn_exp2f(s[e]);
        o = smfmac(lds_ld<u32x4>(va + i * vstep), __builtin_shufflevector(s, s, 0, 1, 2, 3, 4, 5, 6, 7), o, idx);
        o = smfmac(lds_ld<u32x4>(va + i * vstep + 512), __builtin_shufflevector(s, s, 8, 9, 10, 11, 12, 13, 14, 15), o, idx);
    }
}

DI void warm_code(unsigned p, unsigned lane) {
    u64 pc; unsigned back, len;
    asm volatile("s_getpc_b64 %0\n9:\n\ts_mov_b32 %1, 9b-.Lfused_code_begin\n\ts_mov_b32 %2, .Lfused_code_end-.Lfused_code_begin" : "=s"(pc), "=s"(back), "=s"(len));
    if (p * 8192 < len) warm(mkrsrc((const void*)(pc - back), len), lane * 128, p * 8192);
}
struct Parked { u32x4 f[6][8]; };
template <int N> DI void fetch_chunk(const Ctx& c, unsigned lane, u32x4 (&f)[8]) {
    const unsigned src = chunk_src<N>(c);
#pragma unroll
    for (int i = 0; i < 8; ++i) f[i] = ldfrag(c.rws, lane * 16, src, i);
}
DI void attention_side(const Ctx& c, unsigned layer, unsigned lane, Parked& pk) {
    const unsigned hw = c.w - 4, sh = (c.r() >> 2) * 32 + 4 * c.g() + (c.r() & 3), vo = lane * 16, emp = vconst<EMPTY>();
    const u32x4 empty = {emp, emp, emp, emp};
    u32x4 part[8];
    const unsigned q = hw & 1;
    if (layer == 0) {
        const unsigned po = q ? (lane >> 3) * 2048 + (lane & 7) * 16 : (lane >= 28 ? 7168 - 448 : 0) + lane * 16, pitch = q ? 6144 : 14336;
        const unsigned so = (q ? OFF_CSQ + c.col8() * 128 : OFF_CSG + c.col8() * 448) + (hw >> 1) * 8 * pitch;
#pragma unroll
        for (int kb = 0; kb < 8; ++kb) part[kb] = ld16<SC1>(c.rws, po, so + kb * pitch);
        warm_code(c.r() * 4 + hw, lane);
    }
#pragma unroll
    for (int i = 0; i < 7; ++i) stfrag(empty, c.rws, vo, OFF_H + layer * 7 * MB + sh * 28672 + hw * 7168, i);
#pragma unroll
    for (int i = 0; i < 2; ++i) stfrag(empty, c.rws, vo, OFF_X1 + layer * 2 * MB + sh * 8192 + hw * 2048, i);
    if (hw == 0) st16<SC1>(empty, c.rws, vo, OFF_ST1 + layer * 262144 + sh * 1024);
    if (layer == 0) {
        f32x4 s = bc<f32x4>(part[0]);
#pragma unroll
        for (int kb = 1; kb < 8; ++kb) s += bc<f32x4>(part[kb]);
        if (lane < (q ? 24u : 56u)) lds_st<f32x4>(L_CSP + hw * 1024 + lane * 16, s);
    }
    fetch_chunk<0>(c, lane, pk.f[0]); fetch_chunk<1>(c, lane, pk.f[1]);
    fetch_chunk<13>(c, lane, pk.f[2]); fetch_chunk<14>(c, lane, pk.f[3]); fetch_chunk<15>(c, lane, pk.f[4]);
    fetch_chunk<4>(c, lane, pk.f[5]);
}

DI void attention(const Ctx& c0, unsigned layer, Parked& pk) {
    const Ctx c = fresh(c0); const unsigned lane = lane_id();
    const unsigned w = c.w, head = 4 * c.g() + (c.r() & 3), qb = c.r() >> 2, t = lane & 31, h = lane >> 5, kh = w >> 2;
    const unsigned tt = 8 * qb + 2 * (w & 3), t0 = 32 * kh + (w & 3), l16 = lane * 16;
    if (w < 4) { const unsigned v = (t == 1 || t == 5) ? vconst<ONES>() : 0u; lds_st<u32x4>(L_EX + w * 1024 + l16, u32x4{v, v, v, v}); }
    const bool isv = t < 16;
    const unsigned va0 = isv ? L_V + h * 256 + t * 16 : L_EX + (t == 16 ? 16u : t == 20 ? 80u : (t > 20 && t < 28) ? 64u : 0u);
    unsigned ka = kh * 32768 + l16, va = va0 + (isv ? kh * 32768 : 0u);
    const unsigned idx = vconst<0xDDDD>(), hso = layer * 2 * MB + head * 65536;
    unsigned sk = OFF_K + hso + t0 * 1024, sv = OFF_V + hso + t0 * 1024, kd = t0 * 1024, spin, cnt = 3;
    const unsigned wi = c.r() * 8 + w, w0 = OFF_WG + c.half() * (8 * 229376) + wi * 7168;
    const unsigned w1 = wi < 128 ? OFF_WD + c.half() * (4 * 229376) + wi * 7168 : wi < 160 ? OFF_WO + c.half() * 262144 + (wi - 128) * 8192 : (WARM_WQ && layer == 0) ? OFF_WQ + c.half() * (8 * 98304) + (wi - 160) * 8192 : WS_USED;
    if (kh) __builtin_amdgcn_s_setprio(1);
    wait_flags(c.ws, OFF_FQKV + layer * 16384 + (head >> 1) * 1024 + ((lane & 8) ? 64 + (tt ^ (lane & 1)) : t0 + 4 * (lane & 7)) * 8, c.tag);
    STAMP(16 + layer * 16);
    f32x16 oa, ob, sa, sb;
    u32x4 qa, qn, fw;
    u32x8 kf;
    f32x16 vf;
    u64 m2;
    asm volatile(ATT
                 : "={v[48:63]}"(oa), "={v[64:79]}"(ob), "={v[16:31]}"(sa), "={v[32:47]}"(sb), "={v[80:83]}"(qa), "={v[84:87]}"(qn), "={v[88:95]}"(kf), "={v[96:111]}"(vf),
                   "={v[120:123]}"(fw), "+{v112}"(ka), "+{v113}"(va), [sp] "=&s"(spin), [m2] "=&s"(m2), [sk] "+&s"(sk), [sv] "+&s"(sv), [kd] "+&s"(kd), [cnt] "+&s"(cnt)
                 : "{v114}"(isv ? 4096u : 0u), "{v115}"(idx), "{v116}"(l16), "{v117}"(lane * 128), [w0] "s"(w0), [w1] "s"(w1), [wd] "s"(L_SINK), [rs] "s"(c.rws), [sq] "s"(OFF_Q + hso + tt * 1024), [sq1] "s"(OFF_Q + hso + (tt ^ 1) * 1024),
                   [sfw] "s"(OFF_FW + w * 1024), [tag] "s"(c.tag)
                 : "m0", "scc", "vcc", "memory");
    STAMP(18 + layer * 16);
    if (layer == 0 && c.b == 0 && w == 0 && lane == 0) __hip_atomic_store((gu64*)(c.ws + OFF_EPOCH), c.tag, RLX_AGENT);
    const unsigned pa = L_PART + (w & 3) * 4608;
    if (kh) {
        lds_st<f32x4>(pa + l16, f32x4{oa[0], oa[1], oa[2], oa[3]}); lds_st<f32x4>(pa + 1024 + l16, f32x4{oa[4], oa[5], oa[6], oa[7]}); lds_st<float>(pa + 2048 + lane * 4, oa[8]);
        lds_st<f32x4>(pa + 2304 + l16, f32x4{ob[0], ob[1], ob[2], ob[3]}); lds_st<f32x4>(pa + 3328 + l16, f32x4{ob[4], ob[5], ob[6], ob[7]}); lds_st<float>(pa + 4352 + lane * 4, ob[8]);
        attention_side(c, layer, lane, pk);
        __builtin_amdgcn_s_setprio(0);
        __builtin_amdgcn_s_waitcnt(0x0f70);
        vmwait<0>();
        STAMP(17 + layer * 16);
        bar_lds();
    } else {
        bar();
        auto gather = [&](f32x16& o, int x) AI {
            const f32x4 p0 = lds_ld<f32x4>(pa + x * 2304 + l16), p1 = lds_ld<f32x4>(pa + x * 2304 + 1024 + l16);
#pragma unroll
            for (int e = 0; e < 8; ++e) o[e] += e < 4 ? p0[e & 3] : p1[e & 3];
            o[8] += lds_ld<float>(pa + x * 2304 + 2048 + lane * 4);
            const float mag = ((__builtin_fabsf(o[0]) + __builtin_fabsf(o[1])) + (__builtin_fabsf(o[2]) + __builtin_fabsf(o[3]))) +
                              ((__builtin_fabsf(o[4]) + __builtin_fabsf(o[5])) + (__builtin_fabsf(o[6]) + __builtin_fabsf(o[7]))) + o[8];
            return (o[8] >= 9.313225746e-10f) && (mag < __builtin_inff());
        };
        auto store = [&](const f32x16& o, int x) AI {
            const float inv = __builtin_amdgcn_rcpf(o[8]);
            st16<SC1>(pub(u32x4{pk2(o[0] * inv, o[1] * inv), pk2(o[2] * inv, o[3] * inv), pk2(o[4] * inv, o[5] * inv), pk2(o[6] * inv, o[7] * inv)}),
                      c.rws, (t >> 4) * 1024 + (t & 15) * 16 + h * 256, OFF_O + layer * 2 * MB + (tt + x) * 32768 + (head >> 1) * 2048 + (head & 1) * 512);
        };
        const bool oka = gather(oa, 0), okb = gather(ob, 1);
        if (__builtin_expect(all_lanes(oka && okb), 1)) { store(oa, 0); store(ob, 1); }
        else {
#pragma unroll 1
            for (int x = 0; x < 2; ++x) { f32x16 o; attention_true_max(lane, x ? qn : qa, va0, isv ? 1024u : 0u, idx, o); store(o, x); }
        }
#pragma unroll
        for (int i = 0; i < 48; ++i) asm volatile("" : "=v"(pk.f[i >> 3][i & 7]));
    }
    if (layer == 0 && (w == 4 || w == 5) && lane < (w == 4 ? 56u : 24u))
        lds_st<f32x4>((w == 4 ? L_CSG : L_CSQ) + l16, lds_ld<f32x4>(L_CSP + (w - 4) * 1024 + l16) + lds_ld<f32x4>(L_CSP + (w - 2) * 1024 + l16));
    bar_lds();
}

DI void chain_help(const Ctx& c0, unsigned layer, Parked& pk) {
    const Ctx c = fresh(c0); const unsigned lane = lane_id();
    unsigned lo = lane * 16 + (c.w - 4) * 8192, hi = lo + 2 * L_SLOT;
    PIN(lo); PIN(hi);
    constexpr auto set = [](int n) { return n < 2 ? n : n < 6 ? n + 1 : n < 16 ? n - 11 : n < 18 ? n - 16 : 5; };
    auto place = [&](auto nc) AI {
        constexpr int n = decltype(nc)::v, s = set(n);
#pragma unroll
        for (int i = 0; i < 8; ++i) lds_st<u32x4>((n & 2 ? hi : lo) + (n & 1) * L_SLOT + i * 1024, pk.f[s][i]);
    };
    place(IC<0>{}); place(IC<1>{});
    if (layer == 0) {
#pragma unroll
        for (int p = 0; p < 2; ++p) dma<false, 0>(c.rx, (lane >> 3) * 1024 + ((lane & 7) ^ (lane >> 3)) * 16, (32 * c.pair() + 16 * (c.w - 4) + 8 * p) * 1024 + c.fp() * 128, L_RES + (c.w - 4) * 2048 + p * 1024);
    }
    issue_chunk<2, PACE3>(c, lane);
    vmwait<8>();
    bar_lds();
    if (layer == 0) {
        const unsigned hw = c.w - 4, tt = c.pair() + (hw & 1), emp = vconst<EMPTY>();
        const u32x4 empty = {emp, emp, emp, emp};
#pragma unroll
        for (int i = 0; i < 2; ++i) {
            stfrag(empty, c.rws, lane * 16, OFF_X2 + tt * 32768 + c.fp() * 4096 + (hw >> 1) * 2048, i);
            stfrag(empty, c.rws, lane * 16, OFF_O + 2 * MB + tt * 32768 + c.fp() * 4096 + (hw >> 1) * 2048, i);
        }
        if (lane < 16) st16<SC1>(empty, c.rws, lane * 16, OFF_ST2 + tt * 4096 + c.fp() * 512 + (hw >> 1) * 256);
    }
    issue_chunk<3, 0>(c, lane);
    vmwait<8>();
    bar();
    place(IC<4>{});
    issue_chunk<5, PACE>(c, lane);
    vmwait<8>(); bar_lds();
    stats_to_lds<true>(c, OFF_ST1 + layer * 262144, lane);
    auto step = [&](auto nc) AI {
        constexpr int n = decltype(nc)::v, g = n - 6, q = 16 + g / 2;
        if constexpr (n == 6) bar_lds(); else bar();
        if (layer == 0) {
            const unsigned src = chunk_src<q>(c) + (g & 1) * 4096;
#pragma unroll
            for (int i = 0; i < 4; ++i) pk.f[set(q)][4 * (g & 1) + i] = ldfrag(c.rws, lane * 16, src, i);
        }
        issue_chunk<n, PACE2>(c, lane);
        vmwait<8>();
    };
    vmwait<0>();
    step(IC<6>{}); step(IC<7>{}); step(IC<8>{}); step(IC<9>{}); step(IC<10>{}); step(IC<11>{});
    bar(); issue_chunk<12, PACE2>(c, lane);
    vmwait<8>();
    bar(); place(IC<13>{});
    vmwait<0>(); bar_lds(); place(IC<14>{});
    bar_lds(); place(IC<15>{});
    bar_lds();
    bar(); bar(); bar();
    if (layer == 0) {
        place(IC<16>{}); place(IC<17>{}); place(IC<18>{});
        bar_lds();
        stats_to_lds<false>(c, OFF_ST2, lane);
        bar_lds();
        bar();
    }
}

template <int MODE> struct Proj {
    static constexpr int KS = MODE ? 28 : 8, N0 = MODE ? 9 : 0, TR = MODE ? 14 : 8, I0 = MODE ? I0E : I0C, GATE1 = SPACING * (24 - 2 * I0E) - 1, GATE2 = SPACING * (40 - 2 * I0E) - 1;
    u32x4 tok[TR][2], res[2];
    unsigned pr;
    static DI unsigned tso(const Ctx& c, unsigned layer) {
        const unsigned kh = c.w >> 1, tt = c.pair() + (c.w & 1);
        return MODE ? OFF_H + layer * 7 * MB + (tt * 56 + 28 * kh) * 2048 : OFF_O + layer * 2 * MB + (tt * 16 + 8 * kh) * 2048;
    }
    static DI u32x4 tfrag(const Ctx& c, unsigned lane, unsigned so, int i) {
        if (MODE && i >= 16 && i < 24) {
            const u32x2 lo = ld8<SC1>(c.rws, lane * 8 + (i & 3) * 1024, so + (i >> 2) * 4096), hi = ld8<SC1>(c.rws, lane * 8 + (i & 3) * 1024 + 512, so + (i >> 2) * 4096);
            return u32x4{lo.x, lo.y, hi.x, hi.y};
        }
        return ldfrag(c.rws, lane * 16, so, i);
    }
    static DI unsigned probe_off(unsigned lane) {
        const unsigned li = lane & 15;
        return MODE ? (li & 7) * 2048 + 1024 : li * 1024 - (li & 1) * 512;
    }
    DI void first_loads(const Ctx& c, unsigned lane, unsigned so) {
#pragma unroll
        for (int i = 0; i < 2 * I0; ++i) tok[i >> 1][i & 1] = tfrag(c, lane, so, i);
    }
    DI void load_res(const Ctx& c, unsigned layer, unsigned lane) {
        const unsigned tt = c.pair() + (c.w & 1), ft = 2 * c.fp() + (c.w >> 1);
#pragma unroll
        for (int a = 0; a < 2; ++a) res[a] = ldfrag(c.rws, lane * 16, (MODE ? OFF_X1 + layer * 2 * MB : OFF_X2) + tt * 32768 + ft * 2048, a);
    }
    DI void send_probe(const Ctx& c, unsigned layer, unsigned lane) { pr = __builtin_amdgcn_raw_buffer_load_b32(c.rws, probe_off(lane), tso(c, layer), SC1); }
    DI void gate(const Ctx& c, unsigned layer, unsigned lane) { if (__builtin_expect(missing(pr), 0)) await(c.rws, probe_off(lane), tso(c, layer)); }
    template <int I> DI void first_load(const Ctx& c, unsigned layer, unsigned lane) { tok[I >> 1][I & 1] = tfrag(c, lane, tso(c, layer), I); }
    DI void run(const Ctx& c0, unsigned layer, void* out);
};
template <int MODE> DI void Proj<MODE>::run(const Ctx& c0, unsigned layer, void* out) {
    const Ctx c = fresh(c0); const unsigned lane = lane_id();
    constexpr int R = 4, P = 4;
    const unsigned w = c.w, kh = w >> 1, tt = c.pair() + (w & 1), ft = 2 * c.fp() + kh;
    const unsigned cc = lane & 15, q = lane >> 4, vo = lane * 16, so = tso(c, layer);
    auto tload = [&](int s, u32x4& a, u32x4& b) AI { a = tfrag(c, lane, so, 2 * s); b = tfrag(c, lane, so, 2 * s + 1); };
    if (MODE == 0) { await(c.rws, probe_off(lane), so); first_loads(c, lane, so); if (layer) load_res(c, layer, lane); }
    STAMP((MODE ? 24 : 19) + layer * 16);
    if (MODE == 0) bar();
    float r2[2];
    if (MODE) {
#pragma unroll
        for (int a = 0; a < 2; ++a) r2[a] = lds_ld<float>(L_RS + ((tt & 3) * 32 + 16 * a + cc) * 4);
    }
    u32x4 wf[R];
    unsigned wa[4] = {vo + kh * 6144, vo + kh * 2048 + 2048, vo + kh * 6144 + 2 * L_SLOT, vo + kh * 2048 + 2048 + 2 * L_SLOT};
    PIN(wa[0]); PIN(wa[1]); PIN(wa[2]); PIN(wa[3]);
    auto wread = [&](int n) AI { const int s = n >> 2, sl = (N0 + (s >> 2)) & 3; return lds_ld<u32x4>(wa[((n >> 1) & 1) + (sl & 2)] + (sl & 1) * L_SLOT + (s & 3) * 8192 + (n & 1) * 1024); };
#pragma unroll
    for (int n = 0; n < P; ++n) wf[n] = wread(n);
    f32x4 acc[4][2];
    Check ck;
    static_for<0, 3>([&](auto pc) AI { ensure<decltype(pc)::v>(ck, tok[0][0], tok[0][1], [&](u32x4& x, u32x4& y) AI { tload(0, x, y); }); });
    constexpr auto frag_at = [](int m) {
        int n = -SPACING;
        for (int g = 2 * I0; g < 2 * KS; ++g) {
            const int u = g >> 1, ring = 4 * (u - TR) + 3, gate = !MODE ? 0 : u >= 20 ? GATE2 : u >= 12 ? GATE1 : 0;
            n += SPACING; n = n < ring ? ring : n; n = n < gate ? gate : n;
            if (n == m) return g;
        }
        return -1;
    };
    static_for<0, 4 * KS>([&](auto ic) AI {
        constexpr int n = decltype(ic)::v, s = n >> 2, i = n & 3;
        mma<s == 0>(acc[i][0], wf[n % R], tok[s % TR][0]); SB();
        if constexpr (i < 3 && s + 1 < KS) { ensure<i>(ck, tok[(s + 1) % TR][0], tok[(s + 1) % TR][1], [&](u32x4& x, u32x4& y) AI { tload(s + 1, x, y); }); SB(); }
        if constexpr (MODE && (n == GATE1 || n == GATE2)) { if (__builtin_expect(missing(pr), 0)) await(c.rws, ((n == GATE1 ? 12 : 20) + (lane & 7)) * 2048 + 1024, so); }
        mma<s == 0>(acc[i][1], wf[n % R], tok[s % TR][1]); SB();
        if constexpr (n + P < 4 * KS) wf[(n + P) % R] = wread(n + P);
        if constexpr (MODE && (n == GATE1 - 11 || n == GATE2 - 11)) pr = __builtin_amdgcn_raw_buffer_load_b32(c.rws, ((n == GATE1 - 11 ? 12 : 20) + (lane & 7)) * 2048 + 1024, so, SC1);
        if constexpr (MODE && s == 20 && i == 0) load_res(c, layer, lane);
        constexpr int f = frag_at(n);
        if constexpr (f >= 0) tok[(f >> 1) % TR][f & 1] = tfrag(c, lane, so, f);
        SB();
        if constexpr (MODE && (n & 15) == 15 && n + 1 < 4 * KS) { bar(); STAMP2(42 + n / 16); }
    });
    settle();
    STAMP((MODE ? 25 : 20) + layer * 16);
#pragma unroll
    for (int i = 0; i < 4; ++i) lds_st<f32x4>(L_EX + w * 4096 + i * 1024 + vo, acc[2 + (i >> 1)][i & 1]);
    bar_lds();
    if (MODE == 0 && layer == 0) {
#pragma unroll
        for (int a = 0; a < 2; ++a) {
            const unsigned ra = L_RES + (32 * (w & 1) + 16 * a + cc) * 128 + (((kh * 4 + (q >> 1)) ^ (cc & 7)) * 16) + (q & 1) * 8;
            const u32x2 v0 = lds_ld<u32x2>(ra), v1 = lds_ld<u32x2>(ra ^ 32);
            res[a] = u32x4{v0.x, v0.y, v1.x, v1.y};
        }
    }
    f32x4 y[2][2];
#pragma unroll
    for (int u = 0; u < 4; ++u) y[u & 1][u >> 1] = lds_ld<f32x4>(L_EX + (w ^ 2) * 4096 + u * 1024 + vo);
    auto finish = [&](int a, u32x2 (&p)[2]) AI {
#pragma unroll
        for (int i = 0; i < 2; ++i) {
            const f32x4 sum = acc[i][a] + y[a][i];
            y[a][i] = (MODE ? sum * r2[a] : sum) + f32x4{bflo(res[a][2 * i]), bfhi(res[a][2 * i]), bflo(res[a][2 * i + 1]), bfhi(res[a][2 * i + 1])};
            p[i] = u32x2{pk2(y[a][i][0], y[a][i][1]), pk2(y[a][i][2], y[a][i][3])};
        }
    };
    if (MODE == 1 && layer == 1) {
        const rsrc_t ro = mkrsrc(out, SEQ * DM * 2);
#pragma unroll
        for (int a = 0; a < 2; ++a) { u32x2 p[2]; finish(a, p); st16<SC1>(merge16(p[0], p[1]), ro, cc * 1024 + (q & 1) * 32 + (q >> 1) * 16, tt * 32768 + a * 16384 + ft * 64); }
        return;
    }
#pragma unroll
    for (int a = 0; a < 2; ++a) { u32x2 p[2]; finish(a, p); stfrag(pub(u32x4{p[0].x, p[0].y, p[1].x, p[1].y}), c.rws, vo, (MODE ? OFF_X2 : OFF_X1 + layer * 2 * MB) + tt * 32768 + ft * 2048, a); }
    float s1[2], s2[2];
#pragma unroll
    for (int a = 0; a < 2; ++a) {
        const f32x4 u = y[a][0] + y[a][1], v = y[a][0] * y[a][0] + y[a][1] * y[a][1];
        s1[a] = (u[0] + u[1]) + (u[2] + u[3]); s2[a] = (v[0] + v[1]) + (v[2] + v[3]);
    }
    const u32x2 x1 = swap16(bc<unsigned>(s1[0]), bc<unsigned>(s1[1])), x2 = swap16(bc<unsigned>(s2[0]), bc<unsigned>(s2[1]));
    const float t1 = swap32_add(bc<float>(x1.x) + bc<float>(x1.y)), t2 = swap32_add(bc<float>(x2.x) + bc<float>(x2.y));
    if (lane < 32) st8<SC1>(pub(u32x2{bc<unsigned>(t1), bc<unsigned>(t2)}), c.rws, lane * 8, (MODE ? OFF_ST2 : OFF_ST1 + layer * 262144) + tt * 4096 + ft * 256);
}

DI void gateup(const Ctx& c0, unsigned layer, Proj<1>& next) {
    const Ctx c = fresh(c0); const unsigned lane = lane_id();
    constexpr int R = 4, P = 4, I0 = I0D;
    const unsigned w = c.w, cj = c.col8(), tt = c.quad() + w, q = lane >> 4, vo = lane * 16;
    const unsigned tso = OFF_X1 + layer * 2 * MB + tt * 32768;
    u32x4 tok[16][2], wf[R];
    auto tload = [&](int s, u32x4& a, u32x4& b) AI { a = ldfrag(c.rws, vo, tso, 2 * s); b = ldfrag(c.rws, vo, tso, 2 * s + 1); };
    await(c.rws, (lane & 15) * 2048 + 2044, tso);
    STAMP(22 + layer * 16);
#pragma unroll
    for (int s = 0; s < I0; ++s) tload(s, tok[s][0], tok[s][1]);
    bar();
    STAMP2(7);
    unsigned vh = vo + 2 * L_SLOT;
    PIN(vh);
#pragma unroll
    for (int n = 0; n < P; ++n) wf[n] = stream_frag<2>(vo, vh, n);
    f32x4 acc[14][2];
    f32x4 cg, cu;
    float mu[2], y[2], g, u;
    unsigned pk[2][2][2];
    const unsigned ho = OFF_H + layer * 7 * MB + (tt * 56 + 28 * (cj >> 3)) * 2048, pl = (cj >> 1) & 3, par = cj & 1, lcs = L_CSG + q * 16;
    auto sums = [&](int b) AI { const int i = b < 2 ? b : b == 2 ? 6 : b - 1; cg = lds_ld<f32x4>(lcs + i * 64); cu = lds_ld<f32x4>(lcs + 448 + i * 64); };
    auto epi = [&](auto bc_, auto ec_) AI {
        constexpr int b = decltype(bc_)::v, e = decltype(ec_)::v, odd = b == 1 || b == 4 || b == 6, a = e >> 3, r = (e >> 1) & 3;
        if constexpr (!(e & 1)) { g = __builtin_fmaf(-mu[a], cg[r], acc[2 * b][a][r]); u = __builtin_fmaf(-mu[a], cu[r], acc[2 * b + 1][a][r]); PIN(g); PIN(u); }
        else {
            y[r & 1] = g * u; PIN(y[r & 1]);
            if constexpr (r & 1) { pk[odd][a][r >> 1] = pub(pk2(y[0], y[1])); PIN(pk[odd][a][r >> 1]); }
        }
        if constexpr (e == 15) {
            if constexpr (b < 6) sums(b + 1);
            if constexpr (b == 2) {
#pragma unroll
                for (int x = 0; x < 2; ++x) st8<SC1>(u32x2{pk[0][x][0], pk[0][x][1]}, c.rws, lane * 8 + x * 1024, ho + (8 + pl) * 2048 + par * 512);
            } else if constexpr (odd) {
#pragma unroll
                for (int x = 0; x < 2; ++x)
                    st16<SC1>(u32x4{pk[0][x][0], pk[0][x][1], pk[1][x][0], pk[1][x][1]}, c.rws, vo + x * 1024, ho + ((b == 1 ? 0 : b == 4 ? 12 : 20) + 2 * pl + par) * 2048);
            }
        }
    };
    Check ck;
    auto check = [&](auto sc, auto pc) AI { constexpr int s = decltype(sc)::v; ensure<decltype(pc)::v>(ck, tok[s][0], tok[s][1], [&](u32x4& x, u32x4& z) AI { tload(s, x, z); }); };
    static_for<0, 3>([&](auto pc) AI { check(IC<0>{}, pc); });
    static_for<0, 224>([&](auto ic) AI {
        constexpr int n = decltype(ic)::v, k = n < 112 ? n / 14 : 8 + ((n & 15) >> 1), i = n < 112 ? n % 14 : 2 * ((n - 112) >> 4) + (n & 1);
        mma<k == 0>(acc[i][0], wf[n % R], tok[k][0]); SB();
        if constexpr (n < 112) {

            if constexpr (i >= 2 && i < 5 && k < 7) check(IC<k + 1>{}, IC<i - 2>{});
            if constexpr (k >= 4 && i >= 8 && i < 14) check(IC<2 * k + (i - 8) / 3>{}, IC<(i - 8) % 3>{});
        } else if constexpr (n >= 131) epi(IC<(n - 131) / 16>{}, IC<(n - 131) % 16>{});
        if constexpr (n == 126) { sums(0); mu[0] = lds_ld<float>(L_MU + (32 * w + (lane & 15)) * 4); mu[1] = lds_ld<float>(L_MU + (32 * w + 16 + (lane & 15)) * 4); }
        SB();
        mma<k == 0>(acc[i][1], wf[n % R], tok[k][1]); SB();
        if constexpr (n + P < 224) wf[(n + P) % R] = stream_frag<2>(vo, vh, n + P);
        if constexpr (n % SPD == 0 && 2 * I0 + n / SPD < 32) { constexpr int f = 2 * I0 + n / SPD; tok[f >> 1][f & 1] = ldfrag(c.rws, vo, tso, f); }
        if constexpr (n == PROBE_AT) next.send_probe(c, layer, lane);
        SB();
        if constexpr (n % 32 == 31) { bar(); STAMP2(n < 32 ? 8 : n < 64 ? 9 : n < 96 ? 14 : n < 128 ? 17 : n < 160 ? 21 : n < 192 ? 26 : 29); }
    });
    settle();
    STAMP(23 + layer * 16);
    next.gate(c, layer, lane);
    static_for<224, 243>([&](auto ic) AI {
        constexpr int n = decltype(ic)::v;
        if constexpr (n - 224 < EARLY) next.template first_load<n - 224>(c, layer, lane);
        epi(IC<(n - 131) / 16>{}, IC<(n - 131) % 16>{});
    });
    static_for<EARLY, 2 * Proj<1>::I0>([&](auto ic) AI { next.template first_load<decltype(ic)::v>(c, layer, lane); });
}

__global__ void __launch_bounds__(512, 2) fused_layers(const void* x, const void* wqkv, const void* wo, const void* wg, const void* wu, const void* wd, void* out, char* ws) {
    __shared__ __attribute__((aligned(16))) char lds[LDS_BYTES];
    asm volatile("" ::"v"(lds) : "memory");
    Ctx c;
    c.ws = ws; c.b = __builtin_amdgcn_workgroup_id_x(); c.w = rfl(__builtin_amdgcn_workitem_id_x() >> 6);
    c.rws = mkrsrc(ws, WS_USED); c.rx = mkrsrc(x, SEQ * DM * 2); c.rwqkv = mkrsrc(wqkv, DM * 3 * DM * 2);
    asm volatile(".Lfused_code_begin:");
    STAMP(0);
    if (c.w < 4) {
        const u64 e = __hip_atomic_load((gu64*)(ws + OFF_EPOCH), RLX_AGENT) + 1;
        c.tag = ((u64)rfl((unsigned)(e >> 32)) << 32) | rfl((unsigned)e);
        if (c.w == 0 && lane_id() == 0) lds_st<u64>(L_SYNC, c.tag);
        warm_code(c.r() * 4 + c.w, lane_id());
        asm volatile("s_waitcnt lgkmcnt(0)" ::: "memory");
        phaseA_mfma(c);
    } else {
        c.tag = 0;
        phaseA_help(c, wqkv, wo, wg, wu, wd);
        const u64 e = lds_ld<u64>(L_SYNC);
        c.tag = ((u64)rfl((unsigned)(e >> 32)) << 32) | rfl((unsigned)e);
    }
    STAMP(1);
#pragma unroll 1
    for (unsigned layer = 0; layer < 2; ++layer) {
        Parked pk;
        attention(c, layer, pk);
        STAMP(2 + layer * 8);
        if (c.w < 4) {
            { Proj<0> po; po.run(c, layer, out); }
            STAMP(3 + layer * 8);
            Proj<1> pd;
            gateup(c, layer, pd);
            STAMP(4 + layer * 8);
            pd.run(c, layer, out);
            STAMP(5 + layer * 8);
            if (layer == 0) phaseF(c);
            STAMP(6 + layer * 8);
        } else {
            chain_help(c, layer, pk);
        }
    }
    STAMP_OUT();
    asm volatile(".Lfused_code_end:");
}

}

#if defined(__HIP_DEVICE_COMPILE__)
#pragma clang attribute pop
#endif

extern "C" void kernel_launch(void* const* d_in, const int* in_sizes, int n_in, void* d_out, int out_size, void* d_ws, size_t ws_size, hipStream_t stream) {
    (void)in_sizes; (void)n_in; (void)out_size; (void)ws_size;
    fused_layers<<<256, 512, 0, stream>>>(d_in[0], d_in[1], d_in[2], d_in[4], d_in[5], d_in[6], d_out, (char*)d_ws);
}
```
